```python
import torch

def _softmax(x, axis=-1):
    return torch.softmax(x, dim=axis)
def _gelu(x):
    return torch.nn.functional.gelu(x, approximate="tanh")
def _silu(x):
    return torch.nn.functional.silu(x)
def _sigmoid(x):
    return torch.sigmoid(x)
def _layernorm(x, axis=-1, eps=1e-5):
    m = x.mean(dim=axis, keepdim=True)
    v = x.var(dim=axis, keepdim=True, unbiased=False)
    return (x - m) / torch.sqrt(v + eps)
def _scatter_add(tgt, idx, src, axis=0):
    return tgt.clone().index_add_(axis, idx, src)
def _segment_sum(src, seg, n):
    out = torch.zeros((n,) + src.shape[1:], dtype=src.dtype)
    return out.index_add_(0, seg, src)
def _pad(x, pw):
    flat = tuple(v for pair in reversed(pw) for v in pair)
    return torch.nn.functional.pad(x, flat)

kernel_name = 'standalone__dense_activation_mish__prompt'

def setup_inputs():

    _w = (lambda a: (lambda t: t.to(torch.bfloat16) if t.is_floating_point() else t)(torch.as_tensor(a)))
    B, S, D, F = 1, 2048, 512, 1792
    return {
        "x": _w((lambda *s: torch.randn(*s, dtype=torch.bfloat16) * 0.05)(B, S, D)),
        "w1": _w((lambda *s: torch.randn(*s, dtype=torch.bfloat16) * 3.2)(D, F) * (D ** -0.5)),
        "b1": _w((lambda *s: torch.randn(*s, dtype=torch.bfloat16) * 0.05)(F)),
        "w2": _w((lambda *s: torch.randn(*s, dtype=torch.bfloat16) * 0.05)(F, D) * (F ** -0.5)),
    }

def reference(x, w1, b1, w2):
    h = torch.einsum("bsd,df->bsf", x, w1) + b1
    sp = torch.log1p(torch.exp(-torch.abs(h))) + torch.where(h > 0, h, torch.zeros_like(h))
    act = h * torch.tanh(sp)
    return torch.einsum("bsf,fd->bsd", act, w2)
```

```cpp
#include <hip/hip_runtime.h>
#include <hip/hip_bf16.h>
typedef short bf16x8 __attribute__((ext_vector_type(8)));
typedef float f32x4 __attribute__((ext_vector_type(4)));
typedef unsigned short u16;
__device__ __forceinline__ float bf2f(u16 v){ return __uint_as_float(((unsigned)v)<<16); }
__device__ __forceinline__ u16 f2bf(float f){ __hip_bfloat16 b = __float2bfloat16(f); return *(u16*)&b; }
__device__ __forceinline__ float mish(float h){
  float e = __builtin_amdgcn_exp2f(fminf(h*1.44269504f, 40.f));
  float n = e*(e+2.f);
  return h*n*__builtin_amdgcn_rcpf(n+2.f);
}
template<bool ACT>
__global__ __launch_bounds__(256) void gemm(const u16* __restrict__ A, const u16* __restrict__ B, const u16* __restrict__ bias, u16* __restrict__ C, int M, int N, int K){
  __shared__ u16 As[64][40];
  __shared__ u16 Bs[64][40];
  int tid = threadIdx.x, lane = tid&63, wid = tid>>6, wr = wid>>1, wc = wid&1;
  int bm = blockIdx.y*64, bn = blockIdx.x*64;
  f32x4 acc[2][2] = {};
  for (int k0=0;k0<K;k0+=32){
    { int r = tid>>2, c = (tid&3)*8;
      *(bf16x8*)&As[r][c] = *(const bf16x8*)&A[(size_t)(bm+r)*K + k0 + c]; }
    { int kk = tid>>3, c = (tid&7)*8;
      bf16x8 v = *(const bf16x8*)&B[(size_t)(k0+kk)*N + bn + c];
      for (int j=0;j<8;j++) Bs[c+j][kk] = v[j]; }
    __syncthreads();
    bf16x8 a[2], b[2];
    for (int i=0;i<2;i++){ a[i] = *(bf16x8*)&As[wr*32+i*16+(lane&15)][(lane>>4)*8]; b[i] = *(bf16x8*)&Bs[wc*32+i*16+(lane&15)][(lane>>4)*8]; }
    for (int i=0;i<2;i++) for (int j=0;j<2;j++) acc[i][j] = __builtin_amdgcn_mfma_f32_16x16x32_bf16(a[i], b[j], acc[i][j],0,0,0);
    __syncthreads();
  }
  for (int i=0;i<2;i++) for (int j=0;j<2;j++) for (int r=0;r<4;r++){
    int row = bm+wr*32+i*16+(lane>>4)*4+r, col = bn+wc*32+j*16+(lane&15);
    float v = acc[i][j][r];
    if (ACT) v = mish(v + bf2f(bias[col]));
    C[(size_t)row*N+col] = f2bf(v);
  }
}
extern "C" void kernel_launch(void* const* d_in, const int* in_sizes, int n_in,
                              void* d_out, int out_size, void* d_ws, size_t ws_size,
                              hipStream_t stream) {
  const u16* x=(const u16*)d_in[0]; const u16* w1=(const u16*)d_in[1]; const u16* b1=(const u16*)d_in[2]; const u16* w2=(const u16*)d_in[3];
  u16* h=(u16*)d_ws;
  gemm<true><<<dim3(1792/64,2048/64),256,0,stream>>>(x,w1,b1,h,2048,1792,512);
  gemm<false><<<dim3(512/64,2048/64),256,0,stream>>>(h,w2,nullptr,(u16*)d_out,2048,512,1792);
}
```

```cpp
#include <hip/hip_runtime.h>
#include <stdint.h>

typedef short    s16x4  __attribute__((ext_vector_type(4)));
typedef __bf16   bf16x8 __attribute__((ext_vector_type(8)));
typedef __bf16   bf16x2 __attribute__((ext_vector_type(2)));
typedef float    f32x2  __attribute__((ext_vector_type(2)));
typedef float    f32x4  __attribute__((ext_vector_type(4)));
typedef float    f32x16 __attribute__((ext_vector_type(16)));
typedef unsigned u32x4  __attribute__((ext_vector_type(4)));
typedef unsigned u32x16 __attribute__((ext_vector_type(16)));
typedef unsigned long long u64;
#define LAS __attribute__((address_space(3)))
#define GAS __attribute__((address_space(1)))
#define DI __device__ __forceinline__

#define ROT1 0
#define HELP 1
#define STRICT 0
#define CODEPF 0

constexpr int SEQ = 2048, DM = 512, FF = 1792;
constexpr unsigned UNIT = 16384;
constexpr unsigned H_BYTES = 32u * 14u * 16384u;
constexpr unsigned FLAG_OFF = 8u << 20;
constexpr int CODE_SECT = 160;
constexpr unsigned RED_Q = 64u * 272u;

#define STAMP(i)

DI void glds2(unsigned ldsaddr, const char* base, unsigned v0, unsigned v1) {
  asm volatile("s_mov_b32 m0, %0\n\ts_nop 0\n\t"
               "global_load_lds_dwordx4 %2, %1\n\t"
               "global_load_lds_dwordx4 %3, %1 offset:1024"
               :: "s"(ldsaddr), "s"(base), "v"(v0), "v"(v1) : "memory");
}
#define WAIT_BAR(n) asm volatile("s_waitcnt vmcnt(" #n ") lgkmcnt(0)\n\ts_barrier" ::: "memory")
#define SCHED __builtin_amdgcn_sched_barrier(0)

DI bf16x8 tr_frag(LAS char* p0, LAS char* p1) {
  s16x4 a = __builtin_amdgcn_ds_read_tr16_b64_v4i16((LAS s16x4*)p0);
  s16x4 b = __builtin_amdgcn_ds_read_tr16_b64_v4i16((LAS s16x4*)p1);
  return __builtin_bit_cast(bf16x8, __builtin_shufflevector(a, b, 0, 1, 2, 3, 4, 5, 6, 7));
}
DI float mish(float h) {
  float E = __builtin_amdgcn_fmed3f(__builtin_amdgcn_exp2f(__builtin_fmaf(h, 1.44269504089f, -14.f)), 0.f, 1.f);
  float N = E * (E + 0x1p-13f);
  return h * (N * __builtin_amdgcn_rcpf(N + 0x1p-27f));
}
DI unsigned pk2(float a, float b) { return __builtin_bit_cast(unsigned, __builtin_convertvector((f32x2){a, b}, bf16x2)); }

__global__ __launch_bounds__(512, 2) void mlp_fused(const char* __restrict__ x, const char* __restrict__ w1,
                                                    const unsigned* __restrict__ b1, const char* __restrict__ w2,
                                                    char* __restrict__ out, char* ws) {
  __shared__ __attribute__((aligned(1024))) char smem[163840];
  LAS char* sm = (LAS char*)smem;
  const unsigned lds0 = (unsigned)(size_t)sm;
  STAMP(0);
  asm volatile("" :: "s"(x), "s"(w1), "s"(b1), "s"(w2), "s"(out), "s"(ws));
  const int b = blockIdx.x, xcd = b & 7, i = b >> 3;
  const int w = __builtin_amdgcn_readfirstlane(threadIdx.x >> 6), l = threadIdx.x & 63;
  const int r2 = xcd * 4 + (i >> 3), c2 = i & 7;
  const int h5 = l >> 5, blk = (l >> 4) & 1, q4 = (l & 15) >> 2, p = l & 3;
  GAS u64* flags = (GAS u64*)(ws + FLAG_OFF);

  u64 epoch = 0;
  if (w == 0) asm volatile("s_mov_b32 m0, %2\n\ts_nop 0\n\tglobal_load_lds_dword %0, %1 sc1" :: "v"((unsigned)(l & 1) * 4), "s"(ws + FLAG_OFF + 256 * 128), "s"(lds0 + 9 * UNIT) : "memory");
  auto get_epoch = [&]() { const u64 e = *(volatile LAS u64*)(sm + 9 * UNIT);
    epoch = ((u64)(unsigned)__builtin_amdgcn_readfirstlane((unsigned)(e >> 32)) << 32) | (unsigned)__builtin_amdgcn_readfirstlane((unsigned)e); };

  const unsigned vw2 = (16 * w + (l >> 3)) * 1024 + (((l & 7) ^ (((l >> 3) & 2) << 1)) * 16);
  const char* w2c = w2 + c2 * 128;
  const unsigned ldsw = lds0 + w * 2048;
  auto stage_w2 = [&](int st, int unit) { glds2(ldsw + unit * UNIT, w2c + st * 131072, vw2, vw2 + 7168); };
  const __amdgpu_buffer_rsrc_t hrs = __builtin_amdgcn_make_buffer_rsrc(ws, 0, H_BYTES, 0x00020000);

  int slot;
  if (__builtin_expect(i < 28, 1)) {
    const int rt = (xcd >> 1) * 4 + (i & 3), ct = (xcd & 1) * 7 + (i >> 2);
    slot = rt * 16 + ct;
    const int rot = ROT1 ? ((i >> 2) + 2 * (i & 3)) : 0;
    const unsigned vx0 = (16 * w + (l >> 3)) * 1024 + (((l & 7) ^ ((l >> 4) & 7)) * 16);
    const unsigned vx1 = (16 * w + 8 + (l >> 3)) * 1024 + (((l & 7) ^ ((4 + (l >> 4)) & 7)) * 16) - 1024;
    const unsigned vw0 = (8 * w + (l >> 4)) * 3584 + (((l & 15) ^ (((l >> 4) << 2) | ((2 * w) & 3))) * 16);
    const unsigned vw1 = (8 * w + 4 + (l >> 4)) * 3584 + (((l & 15) ^ (((l >> 4) << 2) | ((2 * w + 1) & 3))) * 16) - 1024;
    const char* xb = x + rt * 131072;
    const char* wb = w1 + ct * 256;
    auto stage1 = [&](int s) {
      const int ks = (s + rot) & 7;
      glds2(ldsw + (s % 3) * UNIT, xb + ks * 128, vx0, vx1);
      glds2(ldsw + (3 + s % 3) * UNIT, wb + ks * (64 * 3584), vw0, vw1);
    };
    stage1(0); stage1(1); stage1(2);
    STAMP(1);
    const int fq = w & 3, mh = w >> 2;
    f32x16 bias;
    { u32x16 bp;
      asm volatile("s_load_dwordx16 %0, %1, 0\n\ts_waitcnt lgkmcnt(0)" : "=&s"(bp) : "s"(b1 + (ct * 128 + fq * 32) / 2) : "memory");
#pragma unroll
      for (int g = 0; g < 4; g++) {
        unsigned e0 = bp[4 * g], e1 = bp[4 * g + 1], e2 = bp[4 * g + 2], e3 = bp[4 * g + 3];
        asm("" : "+s"(e0), "+s"(e1), "+s"(e2), "+s"(e3));
        const unsigned lo = h5 ? e2 : e0, hi = h5 ? e3 : e1;
        bias[4 * g] = __uint_as_float(lo << 16); bias[4 * g + 1] = __uint_as_float(lo & 0xffff0000u);
        bias[4 * g + 2] = __uint_as_float(hi << 16); bias[4 * g + 3] = __uint_as_float(hi & 0xffff0000u);
      } }
    f32x16 acc0 = bias, acc1 = bias;
    LAS char* xa[4];
#pragma unroll
    for (int ks = 0; ks < 4; ks++) xa[ks] = sm + (64 * mh + (l & 31)) * 128 + (((2 * ks + h5) ^ ((l >> 1) & 7)) * 16);
    LAS char* wa[2];
#pragma unroll
    for (int t = 0; t < 2; t++)
      wa[t] = sm + 3 * UNIT + (8 * h5 + 4 * t + q4) * 256 + (((4 * fq + 2 * blk + (p >> 1)) ^ ((q4 << 2) | ((2 * h5 + t) & 3))) * 16) + 8 * (p & 1);
#pragma unroll
    for (int s = 0; s < 8; s++) {
      SCHED;
      if (s == 0) { WAIT_BAR(8); } else { WAIT_BAR(4); }
      if (s == 0) { STAMP(2); }
      if (s == 1) get_epoch();
      if (s >= 1 && s <= 5) stage1(s + 2);
      if (s == 6) { stage_w2(0, 6); stage_w2(1, 7); }
      if (s == 7) { stage_w2(2, 8); stage_w2(3, 9); STAMP(3); }
      const unsigned so = (s % 3) * UNIT;
      bf16x8 a[4], b0[4], b1v[4];
#pragma unroll
      for (int ks = 0; ks < 4; ks++) {
        a[ks] = tr_frag(wa[0] + so + ks * 4096, wa[1] + so + ks * 4096);
        b0[ks] = *(LAS bf16x8*)(xa[ks] + so); b1v[ks] = *(LAS bf16x8*)(xa[ks] + so + 4096);
        SCHED;
      }
#pragma unroll
      for (int ks = 0; ks < 4; ks++) {
        acc0 = __builtin_amdgcn_mfma_f32_32x32x16_bf16(a[ks], b0[ks], acc0, 0, 0, 0);
        acc1 = __builtin_amdgcn_mfma_f32_32x32x16_bf16(a[ks], b1v[ks], acc1, 0, 0, 0);
      }
    }
    SCHED;
    STAMP(4);
    { const unsigned hv = l * 16 + (((rt * 2 + mh) * 14 + ct) * 4 + fq) * 4096;
#pragma unroll
      for (int s = 0; s < 2; s++) {
        u32x4 o0, o1;
#pragma unroll
        for (int j = 0; j < 4; j++) {
          o0[j] = pk2(mish(acc0[8 * s + 2 * j]), mish(acc0[8 * s + 2 * j + 1]));
          o1[j] = pk2(mish(acc1[8 * s + 2 * j]), mish(acc1[8 * s + 2 * j + 1]));
        }
        __builtin_amdgcn_raw_buffer_store_b128(o0, hrs, hv + s * 1024, 0, 16);
        __builtin_amdgcn_raw_buffer_store_b128(o1, hrs, hv + 2048 + s * 1024, 0, 16);
        SCHED;
      } }
    STAMP(5);
    stage_w2(4, 2); stage_w2(5, 5); stage_w2(6, 0); stage_w2(7, 3);
    WAIT_BAR(8);
    STAMP(6);
    if (w == 0) { if (l == 0) __hip_atomic_store(flags + slot * 16, epoch + 1, __ATOMIC_RELAXED, __HIP_MEMORY_SCOPE_AGENT); }
    stage_w2(8, 1); stage_w2(9, 4);
  } else {
    const int hid = xcd * 4 + (i - 28);
    slot = (hid >> 1) * 16 + 14 + (hid & 1);
    const char* w2o = w2 + (c2 - 4) * 128;
    constexpr int UN[10] = {6, 7, 8, 9, 2, 5, 0, 3, 1, 4};
#pragma unroll
    for (int st = 0; st < 10; st++) {
      glds2(ldsw + UN[st] * UNIT, w2o + st * 131072, vw2, vw2 + 7168);
      stage_w2(st, UN[st]);
      if (st == 2) {
        asm volatile("s_waitcnt vmcnt(%0)" :: "i"(HELP ? 12 : 6) : "memory");
        get_epoch();
        if (w == 0 && l == 0) __hip_atomic_store(flags + slot * 16, epoch + 1, __ATOMIC_RELAXED, __HIP_MEMORY_SCOPE_AGENT);
      }
    }
  }

  if (w == 0) {
    const u64 tag = epoch + 1;
    const int nk = b == 255 ? 4 : 1;
    GAS u64* fp = flags + (b == 255 ? l : (r2 >> 1) * 16 + (l < 14 ? l : 0)) * 16;
#pragma nounroll
    for (int k = 0; k < nk; k++) {
#pragma nounroll
      for (int spins = 0; spins < (1 << 18); spins++) {
        if (__all(__hip_atomic_load(fp + k * 1024, __ATOMIC_RELAXED, __HIP_MEMORY_SCOPE_AGENT) == tag)) break;
        __builtin_amdgcn_s_sleep(1);
      }
    }
    if (b == 255 && l == 0) __hip_atomic_store(flags + 256 * 16, tag, __ATOMIC_RELAXED, __HIP_MEMORY_SCOPE_AGENT);
  }
  SCHED;
  asm volatile("s_barrier" ::: "memory");
  STAMP(7);

  {
    const int q = w & 3, mb = w >> 2;
    constexpr int DEP = 6;
    const unsigned hv = l * 16 + (q * 2 + mb) * 2048;
    const unsigned hs = r2 * 14 * UNIT;
    u32x4 hf[14][2];
    auto load_h = [&](int st) {
      hf[st][0] = __builtin_amdgcn_raw_buffer_load_b128(hrs, hv, hs + st * UNIT, 16);
      hf[st][1] = __builtin_amdgcn_raw_buffer_load_b128(hrs, hv + 1024, hs + st * UNIT, 16);
    };
#pragma unroll
    for (int st = 0; st < DEP; st++) load_h(st);
    SCHED;
    WAIT_BAR(12);
    STAMP(8);
    LAS char* wa[2];
#pragma unroll
    for (int nb = 0; nb < 2; nb++)
      wa[nb] = sm + (32 * q + 4 * h5 + q4) * 128 + (((4 * nb + 2 * blk + (p >> 1)) ^ ((q4 & 2) << 1)) * 16) + 8 * (p & 1);
    f32x16 acc0 = {}, acc1 = {};
    constexpr int UN[14] = {6, 7, 8, 9, 2, 5, 0, 3, 1, 4, 6, 7, 8, 9};
#pragma unroll
    for (int st = 0; st < 14; st++) {
      SCHED;
      if (st == 4) { asm volatile("s_waitcnt lgkmcnt(0)\n\ts_barrier" ::: "memory");
        stage_w2(10, 6); stage_w2(11, 7); stage_w2(12, 8); stage_w2(13, 9); }
      if (st == 10) { WAIT_BAR(8); STAMP(9); }
      const unsigned uo = UN[st] * UNIT;
      bf16x8 a0[2], a1[2];
#pragma unroll
      for (int s = 0; s < 2; s++) {
        a0[s] = tr_frag(wa[0] + uo + s * 2048, wa[0] + uo + s * 2048 + 1024);
        a1[s] = tr_frag(wa[1] + uo + s * 2048, wa[1] + uo + s * 2048 + 1024);
        SCHED;
      }
#pragma unroll
      for (int s = 0; s < 2; s++) {
        const bf16x8 hb = __builtin_bit_cast(bf16x8, hf[st][s]);
        acc0 = __builtin_amdgcn_mfma_f32_32x32x16_bf16(a0[s], hb, acc0, 0, 0, 0);
        acc1 = __builtin_amdgcn_mfma_f32_32x32x16_bf16(a1[s], hb, acc1, 0, 0, 0);
      }
      if (st + DEP < 14) load_h(st + DEP);
    }
    SCHED;
    STAMP(10);
    LAS char* rw = sm + q * RED_Q + (32 * mb + (l & 31)) * 272 + h5 * 16;
#pragma unroll
    for (int g = 0; g < 4; g++) {
      *(LAS f32x4*)(rw + g * 32) = (f32x4){acc0[4 * g], acc0[4 * g + 1], acc0[4 * g + 2], acc0[4 * g + 3]};
      *(LAS f32x4*)(rw + 128 + g * 32) = (f32x4){acc1[4 * g], acc1[4 * g + 1], acc1[4 * g + 2], acc1[4 * g + 3]};
    }
    asm volatile("s_waitcnt lgkmcnt(0)\n\ts_barrier" ::: "memory");
    LAS char* rr = sm + (8 * w + (l >> 3)) * 272 + (l & 7) * 32;
    f32x4 s0 = *(LAS f32x4*)rr, s1 = *(LAS f32x4*)(rr + 16);
#pragma unroll
    for (int k = 1; k < 4; k++) { s0 += *(LAS f32x4*)(rr + k * RED_Q); s1 += *(LAS f32x4*)(rr + k * RED_Q + 16); }
    const u32x4 o = {pk2(s0[0], s0[1]), pk2(s0[2], s0[3]), pk2(s1[0], s1[1]), pk2(s1[2], s1[3])};
    *(u32x4*)(out + (size_t)(r2 * 64 + 8 * w + (l >> 3)) * 1024 + c2 * 128 + (l & 7) * 16) = o;
  }
}

extern "C" void kernel_launch(void* const* d_in, const int* in_sizes, int n_in, void* d_out, int out_size,
                              void* d_ws, size_t ws_size, hipStream_t stream) {
  mlp_fused<<<256, 512, 0, stream>>>((const char*)d_in[0], (const char*)d_in[1], (const unsigned*)d_in[2],
                                     (const char*)d_in[3], (char*)d_out, (char*)d_ws);
}
```

```cpp
#define STAMPS
#define AMP 0
#define AMPB 0
#include <hip/hip_runtime.h>
#include <stdint.h>

typedef short    s16x4  __attribute__((ext_vector_type(4)));
typedef __bf16   bf16x8 __attribute__((ext_vector_type(8)));
typedef __bf16   bf16x2 __attribute__((ext_vector_type(2)));
typedef float    f32x2  __attribute__((ext_vector_type(2)));
typedef float    f32x4  __attribute__((ext_vector_type(4)));
typedef float    f32x16 __attribute__((ext_vector_type(16)));
typedef unsigned u32x4  __attribute__((ext_vector_type(4)));
typedef unsigned u32x16 __attribute__((ext_vector_type(16)));
typedef unsigned long long u64;
#define LAS __attribute__((address_space(3)))
#define GAS __attribute__((address_space(1)))
#define DI __device__ __forceinline__

#define ROT1 0
#define HELP 1
#define STRICT 0
#define CODEPF 1

constexpr int SEQ = 2048, DM = 512, FF = 1792;
constexpr unsigned UNIT = 16384;
constexpr unsigned H_BYTES = 32u * 14u * 16384u;
constexpr unsigned FLAG_OFF = 8u << 20;
constexpr int CODE_SECT = 160;
constexpr unsigned RED_Q = 64u * 272u;

#define STAMP(i) asm volatile("s_memrealtime %0\n\ts_waitcnt lgkmcnt(0)" : "=s"(ts[i]) :: "memory")

DI void glds2(unsigned ldsaddr, const char* base, unsigned v0, unsigned v1) {
  asm volatile("s_mov_b32 m0, %0\n\ts_nop 0\n\t"
               "global_load_lds_dwordx4 %2, %1\n\t"
               "global_load_lds_dwordx4 %3, %1 offset:1024"
               :: "s"(ldsaddr), "s"(base), "v"(v0), "v"(v1) : "memory");
}
#define WAIT_BAR(n) asm volatile("s_waitcnt vmcnt(" #n ") lgkmcnt(0)\n\ts_barrier" ::: "memory")
#define SCHED __builtin_amdgcn_sched_barrier(0)

DI bf16x8 tr_frag(LAS char* p0, LAS char* p1) {
  s16x4 a = __builtin_amdgcn_ds_read_tr16_b64_v4i16((LAS s16x4*)p0);
  s16x4 b = __builtin_amdgcn_ds_read_tr16_b64_v4i16((LAS s16x4*)p1);
  return __builtin_bit_cast(bf16x8, __builtin_shufflevector(a, b, 0, 1, 2, 3, 4, 5, 6, 7));
}
DI float mish(float h) {
  float E = __builtin_amdgcn_fmed3f(__builtin_amdgcn_exp2f(__builtin_fmaf(h, 1.44269504089f, -14.f)), 0.f, 1.f);
  float N = E * (E + 0x1p-13f);
  return h * (N * __builtin_amdgcn_rcpf(N + 0x1p-27f));
}
DI unsigned pk2(float a, float b) { return __builtin_bit_cast(unsigned, __builtin_convertvector((f32x2){a, b}, bf16x2)); }

__global__ __launch_bounds__(512, 2) void mlp_fused(const char* __restrict__ x, const char* __restrict__ w1,
                                                    const unsigned* __restrict__ b1, const char* __restrict__ w2,
                                                    char* __restrict__ out, char* ws) {
  __shared__ __attribute__((aligned(1024))) char smem[163840];
  LAS char* sm = (LAS char*)smem;
  const unsigned lds0 = (unsigned)(size_t)sm;
  u64 ts[16];
  for (int k = 0; k < 16; k++) ts[k] = 0;
  STAMP(0);
  asm volatile("" :: "s"(x), "s"(w1), "s"(b1), "s"(w2), "s"(out), "s"(ws));
  const int b = blockIdx.x, xcd = b & 7, i = b >> 3;
  const int w = __builtin_amdgcn_readfirstlane(threadIdx.x >> 6), l = threadIdx.x & 63;
  const int r2 = xcd * 4 + (i >> 3), c2 = i & 7;
  const int h5 = l >> 5, blk = (l >> 4) & 1, q4 = (l & 15) >> 2, p = l & 3;
  GAS u64* flags = (GAS u64*)(ws + FLAG_OFF);

  u64 epoch = 0;
  if (w == 0) asm volatile("s_mov_b32 m0, %2\n\ts_nop 0\n\tglobal_load_lds_dword %0, %1 sc1" :: "v"((unsigned)(l & 1) * 4), "s"(ws + FLAG_OFF + 256 * 128), "s"(lds0 + 9 * UNIT) : "memory");
  auto get_epoch = [&]() { const u64 e = *(volatile LAS u64*)(sm + 9 * UNIT);
    epoch = ((u64)(unsigned)__builtin_amdgcn_readfirstlane((unsigned)(e >> 32)) << 32) | (unsigned)__builtin_amdgcn_readfirstlane((unsigned)e); };
  if (w == 1) {
    u64 pc; asm volatile("s_getpc_b64 %0" : "=s"(pc));
    asm volatile("s_mov_b32 m0, %2\n\ts_nop 0\n\tglobal_load_lds_dword %0, %1" :: "v"((unsigned)(min(i * 8 + (l & 7), CODE_SECT - 1) * 64)), "s"(pc), "s"(lds0 + 9 * UNIT + 2048) : "memory");
  }

  const unsigned vw2 = (16 * w + (l >> 3)) * 1024 + (((l & 7) ^ (((l >> 3) & 2) << 1)) * 16);
  const char* w2c = w2 + c2 * 128;
  const unsigned ldsw = lds0 + w * 2048;
  auto stage_w2 = [&](int st, int unit) { glds2(ldsw + unit * UNIT, w2c + st * 131072, vw2, vw2 + 7168); };
  const __amdgpu_buffer_rsrc_t hrs = __builtin_amdgcn_make_buffer_rsrc(ws, 0, H_BYTES, 0x00020000);

  int slot;
  if (__builtin_expect(i < 28, 1)) {
    const int rt = (xcd >> 1) * 4 + (i & 3), ct = (xcd & 1) * 7 + (i >> 2);
    slot = rt * 16 + ct;
    const int rot = ROT1 ? ((i >> 2) + 2 * (i & 3)) : 0;
    const unsigned vx0 = (16 * w + (l >> 3)) * 1024 + (((l & 7) ^ ((l >> 4) & 7)) * 16);
    const unsigned vx1 = (16 * w + 8 + (l >> 3)) * 1024 + (((l & 7) ^ ((4 + (l >> 4)) & 7)) * 16) - 1024;
    const unsigned vw0 = (8 * w + (l >> 4)) * 3584 + (((l & 15) ^ (((l >> 4) << 2) | ((2 * w) & 3))) * 16);
    const unsigned vw1 = (8 * w + 4 + (l >> 4)) * 3584 + (((l & 15) ^ (((l >> 4) << 2) | ((2 * w + 1) & 3))) * 16) - 1024;
    const char* xb = x + rt * 131072;
    const char* wb = w1 + ct * 256;
    auto stage1 = [&](int s) {
      const int ks = (s + rot) & 7;
      glds2(ldsw + (s % 3) * UNIT, xb + ks * 128, vx0, vx1);
      glds2(ldsw + (3 + s % 3) * UNIT, wb + ks * (64 * 3584), vw0, vw1);
    };
    stage1(0); stage1(1); stage1(2);
    STAMP(1);
    const int fq = w & 3, mh = w >> 2;
    f32x16 bias;
    { u32x16 bp;
      asm volatile("s_load_dwordx16 %0, %1, 0\n\ts_waitcnt lgkmcnt(0)" : "=&s"(bp) : "s"(b1 + (ct * 128 + fq * 32) / 2) : "memory");
#pragma unroll
      for (int g = 0; g < 4; g++) {
        unsigned e0 = bp[4 * g], e1 = bp[4 * g + 1], e2 = bp[4 * g + 2], e3 = bp[4 * g + 3];
        asm("" : "+s"(e0), "+s"(e1), "+s"(e2), "+s"(e3));
        const unsigned lo = h5 ? e2 : e0, hi = h5 ? e3 : e1;
        bias[4 * g] = __uint_as_float(lo << 16); bias[4 * g + 1] = __uint_as_float(lo & 0xffff0000u);
        bias[4 * g + 2] = __uint_as_float(hi << 16); bias[4 * g + 3] = __uint_as_float(hi & 0xffff0000u);
      } }
    f32x16 acc0 = bias, acc1 = bias;
    LAS char* xa[4];
#pragma unroll
    for (int ks = 0; ks < 4; ks++) xa[ks] = sm + (64 * mh + (l & 31)) * 128 + (((2 * ks + h5) ^ ((l >> 1) & 7)) * 16);
    LAS char* wa[2];
#pragma unroll
    for (int t = 0; t < 2; t++)
      wa[t] = sm + 3 * UNIT + (8 * h5 + 4 * t + q4) * 256 + (((4 * fq + 2 * blk + (p >> 1)) ^ ((q4 << 2) | ((2 * h5 + t) & 3))) * 16) + 8 * (p & 1);
#pragma unroll
    for (int s = 0; s < 8; s++) {
      SCHED;
      if (s == 0) { WAIT_BAR(8); } else { WAIT_BAR(4); }
      if (s == 0) { STAMP(2); }
      if (s == 1) get_epoch();
      if (s >= 1 && s <= 5) stage1(s + 2);
      if (s == 6) { stage_w2(0, 6); stage_w2(1, 7); }
      if (s == 7) { stage_w2(2, 8); stage_w2(3, 9); STAMP(3); }
      const unsigned so = (s % 3) * UNIT;
      bf16x8 a[4], b0[4], b1v[4];
#pragma unroll
      for (int ks = 0; ks < 4; ks++) {
        a[ks] = tr_frag(wa[0] + so + ks * 4096, wa[1] + so + ks * 4096);
        b0[ks] = *(LAS bf16x8*)(xa[ks] + so); b1v[ks] = *(LAS bf16x8*)(xa[ks] + so + 4096);
        SCHED;
      }
#pragma unroll
      for (int ks = 0; ks < 4; ks++) {
        acc0 = __builtin_amdgcn_mfma_f32_32x32x16_bf16(a[ks], b0[ks], acc0, 0, 0, 0);
        acc1 = __builtin_amdgcn_mfma_f32_32x32x16_bf16(a[ks], b1v[ks], acc1, 0, 0, 0);
      }
    }
    SCHED;
    STAMP(4);
    { const unsigned hv = l * 16 + (((rt * 2 + mh) * 14 + ct) * 4 + fq) * 4096;
#pragma unroll
      for (int s = 0; s < 2; s++) {
        u32x4 o0, o1;
#pragma unroll
        for (int j = 0; j < 4; j++) {
          o0[j] = pk2(mish(acc0[8 * s + 2 * j]), mish(acc0[8 * s + 2 * j + 1]));
          o1[j] = pk2(mish(acc1[8 * s + 2 * j]), mish(acc1[8 * s + 2 * j + 1]));
        }
        __builtin_amdgcn_raw_buffer_store_b128(o0, hrs, hv + s * 1024, 0, 16);
        __builtin_amdgcn_raw_buffer_store_b128(o1, hrs, hv + 2048 + s * 1024, 0, 16);
        SCHED;
      } }
    STAMP(5);
    stage_w2(4, 2); stage_w2(5, 5); stage_w2(6, 0); stage_w2(7, 3);
    WAIT_BAR(8);
    STAMP(6);
    if (w == 0) { if (l == 0) __hip_atomic_store(flags + slot * 16, epoch + 1, __ATOMIC_RELAXED, __HIP_MEMORY_SCOPE_AGENT); }
    stage_w2(8, 1); stage_w2(9, 4);
  } else {
    const int hid = xcd * 4 + (i - 28);
    slot = (hid >> 1) * 16 + 14 + (hid & 1);
    const char* w2o = w2 + (c2 - 4) * 128;
    constexpr int UN[10] = {6, 7, 8, 9, 2, 5, 0, 3, 1, 4};
#pragma unroll
    for (int st = 0; st < 10; st++) {
      glds2(ldsw + UN[st] * UNIT, w2o + st * 131072, vw2, vw2 + 7168);
      stage_w2(st, UN[st]);
      if (st == 2) {
        asm volatile("s_waitcnt vmcnt(%0)" :: "i"(HELP ? 12 : 6) : "memory");
        get_epoch();
        if (w == 0 && l == 0) __hip_atomic_store(flags + slot * 16, epoch + 1, __ATOMIC_RELAXED, __HIP_MEMORY_SCOPE_AGENT);
      }
    }
  }

  if (w == 0) {
    const u64 tag = epoch + 1;
    const int nk = b == 255 ? 4 : 1;
    GAS u64* fp = flags + (b == 255 ? l : (r2 >> 1) * 16 + (l < 14 ? l : 0)) * 16;
#pragma nounroll
    for (int k = 0; k < nk; k++) {
#pragma nounroll
      for (int spins = 0; spins < (1 << 18); spins++) {
        if (__all(__hip_atomic_load(fp + k * 1024, __ATOMIC_RELAXED, __HIP_MEMORY_SCOPE_AGENT) == tag)) break;
        __builtin_amdgcn_s_sleep(1);
      }
    }
    if (b == 255 && l == 0) __hip_atomic_store(flags + 256 * 16, tag, __ATOMIC_RELAXED, __HIP_MEMORY_SCOPE_AGENT);
  }
  SCHED;
  asm volatile("s_barrier" ::: "memory");
  STAMP(7);

  {
    const int q = w & 3, mb = w >> 2;
    constexpr int DEP = 6;
    const unsigned hv = l * 16 + (q * 2 + mb) * 2048;
    const unsigned hs = r2 * 14 * UNIT;
    u32x4 hf[14][2];
    auto load_h = [&](int st) {
      hf[st][0] = __builtin_amdgcn_raw_buffer_load_b128(hrs, hv, hs + st * UNIT, 16);
      hf[st][1] = __builtin_amdgcn_raw_buffer_load_b128(hrs, hv + 1024, hs + st * UNIT, 16);
    };
#pragma unroll
    for (int st = 0; st < DEP; st++) load_h(st);
    SCHED;
    WAIT_BAR(12);
    STAMP(8);
    LAS char* wa[2];
#pragma unroll
    for (int nb = 0; nb < 2; nb++)
      wa[nb] = sm + (32 * q + 4 * h5 + q4) * 128 + (((4 * nb + 2 * blk + (p >> 1)) ^ ((q4 & 2) << 1)) * 16) + 8 * (p & 1);
    f32x16 acc0 = {}, acc1 = {};
    constexpr int UN[14] = {6, 7, 8, 9, 2, 5, 0, 3, 1, 4, 6, 7, 8, 9};
#pragma unroll
    for (int st = 0; st < 14; st++) {
      SCHED;
      if (st == 4) { asm volatile("s_waitcnt lgkmcnt(0)\n\ts_barrier" ::: "memory");
        stage_w2(10, 6); stage_w2(11, 7); stage_w2(12, 8); stage_w2(13, 9); }
      if (st == 10) { WAIT_BAR(8); STAMP(9); }
      const unsigned uo = UN[st] * UNIT;
      bf16x8 a0[2], a1[2];
#pragma unroll
      for (int s = 0; s < 2; s++) {
        a0[s] = tr_frag(wa[0] + uo + s * 2048, wa[0] + uo + s * 2048 + 1024);
        a1[s] = tr_frag(wa[1] + uo + s * 2048, wa[1] + uo + s * 2048 + 1024);
        SCHED;
      }
#pragma unroll
      for (int s = 0; s < 2; s++) {
        const bf16x8 hb = __builtin_bit_cast(bf16x8, hf[st][s]);
        acc0 = __builtin_amdgcn_mfma_f32_32x32x16_bf16(a0[s], hb, acc0, 0, 0, 0);
        acc1 = __builtin_amdgcn_mfma_f32_32x32x16_bf16(a1[s], hb, acc1, 0, 0, 0);
      }
      if (st + DEP < 14) load_h(st + DEP);
    }
    SCHED;
    STAMP(10);
    LAS char* rw = sm + q * RED_Q + (32 * mb + (l & 31)) * 272 + h5 * 16;
#pragma unroll
    for (int g = 0; g < 4; g++) {
      *(LAS f32x4*)(rw + g * 32) = (f32x4){acc0[4 * g], acc0[4 * g + 1], acc0[4 * g + 2], acc0[4 * g + 3]};
      *(LAS f32x4*)(rw + 128 + g * 32) = (f32x4){acc1[4 * g], acc1[4 * g + 1], acc1[4 * g + 2], acc1[4 * g + 3]};
    }
    asm volatile("s_waitcnt lgkmcnt(0)\n\ts_barrier" ::: "memory");
    LAS char* rr = sm + (8 * w + (l >> 3)) * 272 + (l & 7) * 32;
    f32x4 s0 = *(LAS f32x4*)rr, s1 = *(LAS f32x4*)(rr + 16);
#pragma unroll
    for (int k = 1; k < 4; k++) { s0 += *(LAS f32x4*)(rr + k * RED_Q); s1 += *(LAS f32x4*)(rr + k * RED_Q + 16); }
    const u32x4 o = {pk2(s0[0], s0[1]), pk2(s0[2], s0[3]), pk2(s1[0], s1[1]), pk2(s1[2], s1[3])};
    *(u32x4*)(out + (size_t)(r2 * 64 + 8 * w + (l >> 3)) * 1024 + c2 * 128 + (l & 7) * 16) = o;
  }
  STAMP(11);
  asm volatile("s_waitcnt vmcnt(0)" ::: "memory");
  STAMP(12);
  if (b == AMPB && w == 0) { const u64 tgt = ts[12] + 8 * (ts[AMP] - ts[0]); u64 t1; do { asm volatile("s_memrealtime %0\n\ts_waitcnt lgkmcnt(0)" : "=s"(t1) :: "memory"); } while (t1 < tgt); }
}

extern "C" void kernel_launch(void* const* d_in, const int* in_sizes, int n_in, void* d_out, int out_size,
                              void* d_ws, size_t ws_size, hipStream_t stream) {
  mlp_fused<<<256, 512, 0, stream>>>((const char*)d_in[0], (const char*)d_in[1], (const unsigned*)d_in[2],
                                     (const char*)d_in[3], (char*)d_out, (char*)d_ws);
}
```

```cpp
#include <hip/hip_runtime.h>
#include <stdint.h>

typedef short    s16x4  __attribute__((ext_vector_type(4)));
typedef __bf16   bf16x8 __attribute__((ext_vector_type(8)));
typedef __bf16   bf16x2 __attribute__((ext_vector_type(2)));
typedef float    f32x2  __attribute__((ext_vector_type(2)));
typedef float    f32x4  __attribute__((ext_vector_type(4)));
typedef float    f32x16 __attribute__((ext_vector_type(16)));
typedef unsigned u32x4  __attribute__((ext_vector_type(4)));
typedef unsigned u32x16 __attribute__((ext_vector_type(16)));
typedef unsigned long long u64;
#define LAS __attribute__((address_space(3)))
#define GAS __attribute__((address_space(1)))
#define DI __device__ __forceinline__

#define ROT1 0
#define HELP 1
#define STRICT 0
#define CODEPF 1

constexpr int SEQ = 2048, DM = 512, FF = 1792;
constexpr unsigned UNIT = 16384;
constexpr unsigned H_BYTES = 32u * 14u * 16384u;
constexpr unsigned FLAG_OFF = 8u << 20;
constexpr int CODE_SECT = 160;
constexpr unsigned RED_Q = 64u * 272u;

#define STAMP(i)

DI void glds2(unsigned ldsaddr, const char* base, unsigned v0, unsigned v1) {
  asm volatile("s_mov_b32 m0, %0\n\ts_nop 0\n\t"
               "global_load_lds_dwordx4 %2, %1\n\t"
               "global_load_lds_dwordx4 %3, %1 offset:1024"
               :: "s"(ldsaddr), "s"(base), "v"(v0), "v"(v1) : "memory");
}
#define WAIT_BAR(n) asm volatile("s_waitcnt vmcnt(" #n ") lgkmcnt(0)\n\ts_barrier" ::: "memory")
#define SCHED __builtin_amdgcn_sched_barrier(0)

DI bf16x8 tr_frag(LAS char* p0, LAS char* p1) {
  s16x4 a = __builtin_amdgcn_ds_read_tr16_b64_v4i16((LAS s16x4*)p0);
  s16x4 b = __builtin_amdgcn_ds_read_tr16_b64_v4i16((LAS s16x4*)p1);
  return __builtin_bit_cast(bf16x8, __builtin_shufflevector(a, b, 0, 1, 2, 3, 4, 5, 6, 7));
}
DI float mish(float h) {
  float E = __builtin_amdgcn_fmed3f(__builtin_amdgcn_exp2f(__builtin_fmaf(h, 1.44269504089f, -14.f)), 0.f, 1.f);
  float N = E * (E + 0x1p-13f);
  return h * (N * __builtin_amdgcn_rcpf(N + 0x1p-27f));
}
DI unsigned pk2(float a, float b) { return __builtin_bit_cast(unsigned, __builtin_convertvector((f32x2){a, b}, bf16x2)); }

__global__ __launch_bounds__(512, 2) void mlp_fused(const char* __restrict__ x, const char* __restrict__ w1,
                                                    const unsigned* __restrict__ b1, const char* __restrict__ w2,
                                                    char* __restrict__ out, char* ws) {
  __shared__ __attribute__((aligned(1024))) char smem[163840];
  LAS char* sm = (LAS char*)smem;
  const unsigned lds0 = (unsigned)(size_t)sm;
  STAMP(0);
  asm volatile("" :: "s"(x), "s"(w1), "s"(b1), "s"(w2), "s"(out), "s"(ws));
  const int b = blockIdx.x, xcd = b & 7, i = b >> 3;
  const int w = __builtin_amdgcn_readfirstlane(threadIdx.x >> 6), l = threadIdx.x & 63;
  const int r2 = xcd * 4 + (i >> 3), c2 = i & 7;
  const int h5 = l >> 5, blk = (l >> 4) & 1, q4 = (l & 15) >> 2, p = l & 3;
  GAS u64* flags = (GAS u64*)(ws + FLAG_OFF);

  u64 epoch = 0;
  if (w == 0) asm volatile("s_mov_b32 m0, %2\n\ts_nop 0\n\tglobal_load_lds_dword %0, %1 sc1" :: "v"((unsigned)(l & 1) * 4), "s"(ws + FLAG_OFF + 256 * 128), "s"(lds0 + 9 * UNIT) : "memory");
  auto get_epoch = [&]() { const u64 e = *(volatile LAS u64*)(sm + 9 * UNIT);
    epoch = ((u64)(unsigned)__builtin_amdgcn_readfirstlane((unsigned)(e >> 32)) << 32) | (unsigned)__builtin_amdgcn_readfirstlane((unsigned)e); };
  if (w == 1) {
    u64 pc; asm volatile("s_getpc_b64 %0" : "=s"(pc));
    asm volatile("s_mov_b32 m0, %2\n\ts_nop 0\n\tglobal_load_lds_dword %0, %1" :: "v"((unsigned)(min(i * 8 + (l & 7), CODE_SECT - 1) * 64)), "s"(pc), "s"(lds0 + 9 * UNIT + 2048) : "memory");
  }

  const unsigned vw2 = (16 * w + (l >> 3)) * 1024 + (((l & 7) ^ (((l >> 3) & 2) << 1)) * 16);
  const char* w2c = w2 + c2 * 128;
  const unsigned ldsw = lds0 + w * 2048;
  auto stage_w2 = [&](int st, int unit) { glds2(ldsw + unit * UNIT, w2c + st * 131072, vw2, vw2 + 7168); };
  const __amdgpu_buffer_rsrc_t hrs = __builtin_amdgcn_make_buffer_rsrc(ws, 0, H_BYTES, 0x00020000);

  int slot;
  if (__builtin_expect(i < 28, 1)) {
    const int rt = (xcd >> 1) * 4 + (i & 3), ct = (xcd & 1) * 7 + (i >> 2);
    slot = rt * 16 + ct;
    const int rot = ROT1 ? ((i >> 2) + 2 * (i & 3)) : 0;
    const unsigned vx0 = (16 * w + (l >> 3)) * 1024 + (((l & 7) ^ ((l >> 4) & 7)) * 16);
    const unsigned vx1 = (16 * w + 8 + (l >> 3)) * 1024 + (((l & 7) ^ ((4 + (l >> 4)) & 7)) * 16) - 1024;
    const unsigned vw0 = (8 * w + (l >> 4)) * 3584 + (((l & 15) ^ (((l >> 4) << 2) | ((2 * w) & 3))) * 16);
    const unsigned vw1 = (8 * w + 4 + (l >> 4)) * 3584 + (((l & 15) ^ (((l >> 4) << 2) | ((2 * w + 1) & 3))) * 16) - 1024;
    const char* xb = x + rt * 131072;
    const char* wb = w1 + ct * 256;
    auto stage1 = [&](int s) {
      const int ks = (s + rot) & 7;
      glds2(ldsw + (s % 3) * UNIT, xb + ks * 128, vx0, vx1);
      glds2(ldsw + (3 + s % 3) * UNIT, wb + ks * (64 * 3584), vw0, vw1);
    };
    stage1(0); stage1(1); stage1(2);
    STAMP(1);
    const int fq = w & 3, mh = w >> 2;
    f32x16 bias;
    { u32x16 bp;
      asm volatile("s_load_dwordx16 %0, %1, 0\n\ts_waitcnt lgkmcnt(0)" : "=&s"(bp) : "s"(b1 + (ct * 128 + fq * 32) / 2) : "memory");
#pragma unroll
      for (int g = 0; g < 4; g++) {
        unsigned e0 = bp[4 * g], e1 = bp[4 * g + 1], e2 = bp[4 * g + 2], e3 = bp[4 * g + 3];
        asm("" : "+s"(e0), "+s"(e1), "+s"(e2), "+s"(e3));
        const unsigned lo = h5 ? e2 : e0, hi = h5 ? e3 : e1;
        bias[4 * g] = __uint_as_float(lo << 16); bias[4 * g + 1] = __uint_as_float(lo & 0xffff0000u);
        bias[4 * g + 2] = __uint_as_float(hi << 16); bias[4 * g + 3] = __uint_as_float(hi & 0xffff0000u);
      } }
    f32x16 acc0 = bias, acc1 = bias;
    LAS char* xa[4];
#pragma unroll
    for (int ks = 0; ks < 4; ks++) xa[ks] = sm + (64 * mh + (l & 31)) * 128 + (((2 * ks + h5) ^ ((l >> 1) & 7)) * 16);
    LAS char* wa[2];
#pragma unroll
    for (int t = 0; t < 2; t++)
      wa[t] = sm + 3 * UNIT + (8 * h5 + 4 * t + q4) * 256 + (((4 * fq + 2 * blk + (p >> 1)) ^ ((q4 << 2) | ((2 * h5 + t) & 3))) * 16) + 8 * (p & 1);
#pragma unroll
    for (int s = 0; s < 8; s++) {
      SCHED;
      if (s == 0) { WAIT_BAR(8); } else { WAIT_BAR(4); }
      if (s == 0) { STAMP(2); }
      if (s == 1) get_epoch();
      if (s >= 1 && s <= 5) stage1(s + 2);
      if (s == 6) { stage_w2(0, 6); stage_w2(1, 7); }
      if (s == 7) { stage_w2(2, 8); stage_w2(3, 9); STAMP(3); }
      const unsigned so = (s % 3) * UNIT;
      bf16x8 a[4], b0[4], b1v[4];
#pragma unroll
      for (int ks = 0; ks < 4; ks++) {
        a[ks] = tr_frag(wa[0] + so + ks * 4096, wa[1] + so + ks * 4096);
        b0[ks] = *(LAS bf16x8*)(xa[ks] + so); b1v[ks] = *(LAS bf16x8*)(xa[ks] + so + 4096);
        SCHED;
      }
#pragma unroll
      for (int ks = 0; ks < 4; ks++) {
        acc0 = __builtin_amdgcn_mfma_f32_32x32x16_bf16(a[ks], b0[ks], acc0, 0, 0, 0);
        acc1 = __builtin_amdgcn_mfma_f32_32x32x16_bf16(a[ks], b1v[ks], acc1, 0, 0, 0);
      }
    }
    SCHED;
    STAMP(4);
    { const unsigned hv = l * 16 + (((rt * 2 + mh) * 14 + ct) * 4 + fq) * 4096;
#pragma unroll
      for (int s = 0; s < 2; s++) {
        u32x4 o0, o1;
#pragma unroll
        for (int j = 0; j < 4; j++) {
          o0[j] = pk2(mish(acc0[8 * s + 2 * j]), mish(acc0[8 * s + 2 * j + 1]));
          o1[j] = pk2(mish(acc1[8 * s + 2 * j]), mish(acc1[8 * s + 2 * j + 1]));
        }
        __builtin_amdgcn_raw_buffer_store_b128(o0, hrs, hv + s * 1024, 0, 16);
        __builtin_amdgcn_raw_buffer_store_b128(o1, hrs, hv + 2048 + s * 1024, 0, 16);
        SCHED;
      } }
    STAMP(5);
    stage_w2(4, 2); stage_w2(5, 5); stage_w2(6, 0); stage_w2(7, 3);
    WAIT_BAR(8);
    STAMP(6);
    if (w == 0) { if (l == 0) __hip_atomic_store(flags + slot * 16, epoch + 1, __ATOMIC_RELAXED, __HIP_MEMORY_SCOPE_AGENT); }
    stage_w2(8, 1); stage_w2(9, 4);
  } else {
    const int hid = xcd * 4 + (i - 28);
    slot = (hid >> 1) * 16 + 14 + (hid & 1);
    const char* w2o = w2 + (c2 - 4) * 128;
    constexpr int UN[10] = {6, 7, 8, 9, 2, 5, 0, 3, 1, 4};
#pragma unroll
    for (int st = 0; st < 10; st++) {
      glds2(ldsw + UN[st] * UNIT, w2o + st * 131072, vw2, vw2 + 7168);
      stage_w2(st, UN[st]);
      if (st == 2) {
        asm volatile("s_waitcnt vmcnt(%0)" :: "i"(HELP ? 12 : 6) : "memory");
        get_epoch();
        if (w == 0 && l == 0) __hip_atomic_store(flags + slot * 16, epoch + 1, __ATOMIC_RELAXED, __HIP_MEMORY_SCOPE_AGENT);
      }
    }
  }

  if (w == 0) {
    const u64 tag = epoch + 1;
    const int nk = b == 255 ? 4 : 1;
    GAS u64* fp = flags + (b == 255 ? l : (r2 >> 1) * 16 + (l < 14 ? l : 0)) * 16;
#pragma nounroll
    for (int k = 0; k < nk; k++) {
#pragma nounroll
      for (int spins = 0; spins < (1 << 18); spins++) {
        if (__all(__hip_atomic_load(fp + k * 1024, __ATOMIC_RELAXED, __HIP_MEMORY_SCOPE_AGENT) == tag)) break;
        __builtin_amdgcn_s_sleep(1);
      }
    }
    if (b == 255 && l == 0) __hip_atomic_store(flags + 256 * 16, tag, __ATOMIC_RELAXED, __HIP_MEMORY_SCOPE_AGENT);
  }
  SCHED;
  asm volatile("s_barrier" ::: "memory");
  STAMP(7);

  {
    const int q = w & 3, mb = w >> 2;
    constexpr int DEP = 6;
    const unsigned hv = l * 16 + (q * 2 + mb) * 2048;
    const unsigned hs = r2 * 14 * UNIT;
    u32x4 hf[14][2];
    auto load_h = [&](int st) {
      hf[st][0] = __builtin_amdgcn_raw_buffer_load_b128(hrs, hv, hs + st * UNIT, 16);
      hf[st][1] = __builtin_amdgcn_raw_buffer_load_b128(hrs, hv + 1024, hs + st * UNIT, 16);
    };
#pragma unroll
    for (int st = 0; st < DEP; st++) load_h(st);
    SCHED;
    WAIT_BAR(12);
    STAMP(8);
    LAS char* wa[2];
#pragma unroll
    for (int nb = 0; nb < 2; nb++)
      wa[nb] = sm + (32 * q + 4 * h5 + q4) * 128 + (((4 * nb + 2 * blk + (p >> 1)) ^ ((q4 & 2) << 1)) * 16) + 8 * (p & 1);
    f32x16 acc0 = {}, acc1 = {};
    constexpr int UN[14] = {6, 7, 8, 9, 2, 5, 0, 3, 1, 4, 6, 7, 8, 9};
#pragma unroll
    for (int st = 0; st < 14; st++) {
      SCHED;
      if (st == 4) { asm volatile("s_waitcnt lgkmcnt(0)\n\ts_barrier" ::: "memory");
        stage_w2(10, 6); stage_w2(11, 7); stage_w2(12, 8); stage_w2(13, 9); }
      if (st == 10) { WAIT_BAR(8); STAMP(9); }
      const unsigned uo = UN[st] * UNIT;
      bf16x8 a0[2], a1[2];
#pragma unroll
      for (int s = 0; s < 2; s++) {
        a0[s] = tr_frag(wa[0] + uo + s * 2048, wa[0] + uo + s * 2048 + 1024);
        a1[s] = tr_frag(wa[1] + uo + s * 2048, wa[1] + uo + s * 2048 + 1024);
        SCHED;
      }
#pragma unroll
      for (int s = 0; s < 2; s++) {
        const bf16x8 hb = __builtin_bit_cast(bf16x8, hf[st][s]);
        acc0 = __builtin_amdgcn_mfma_f32_32x32x16_bf16(a0[s], hb, acc0, 0, 0, 0);
        acc1 = __builtin_amdgcn_mfma_f32_32x32x16_bf16(a1[s], hb, acc1, 0, 0, 0);
      }
      if (st + DEP < 14) load_h(st + DEP);
    }
    SCHED;
    STAMP(10);
    LAS char* rw = sm + q * RED_Q + (32 * mb + (l & 31)) * 272 + h5 * 16;
#pragma unroll
    for (int g = 0; g < 4; g++) {
      *(LAS f32x4*)(rw + g * 32) = (f32x4){acc0[4 * g], acc0[4 * g + 1], acc0[4 * g + 2], acc0[4 * g + 3]};
      *(LAS f32x4*)(rw + 128 + g * 32) = (f32x4){acc1[4 * g], acc1[4 * g + 1], acc1[4 * g + 2], acc1[4 * g + 3]};
    }
    asm volatile("s_waitcnt lgkmcnt(0)\n\ts_barrier" ::: "memory");
    LAS char* rr = sm + (8 * w + (l >> 3)) * 272 + (l & 7) * 32;
    f32x4 s0 = *(LAS f32x4*)rr, s1 = *(LAS f32x4*)(rr + 16);
#pragma unroll
    for (int k = 1; k < 4; k++) { s0 += *(LAS f32x4*)(rr + k * RED_Q); s1 += *(LAS f32x4*)(rr + k * RED_Q + 16); }
    const u32x4 o = {pk2(s0[0], s0[1]), pk2(s0[2], s0[3]), pk2(s1[0], s1[1]), pk2(s1[2], s1[3])};
    *(u32x4*)(out + (size_t)(r2 * 64 + 8 * w + (l >> 3)) * 1024 + c2 * 128 + (l & 7) * 16) = o;
  }
}

extern "C" void kernel_launch(void* const* d_in, const int* in_sizes, int n_in, void* d_out, int out_size,
                              void* d_ws, size_t ws_size, hipStream_t stream) {
  mlp_fused<<<256, 512, 0, stream>>>((const char*)d_in[0], (const char*)d_in[1], (const unsigned*)d_in[2],
                                     (const char*)d_in[3], (char*)d_out, (char*)d_ws);
}
```

```cpp
#define STAMPS
#define AMP 0
#define AMPB 0
#include <hip/hip_runtime.h>
#include <stdint.h>

typedef short    s16x4  __attribute__((ext_vector_type(4)));
typedef __bf16   bf16x8 __attribute__((ext_vector_type(8)));
typedef __bf16   bf16x2 __attribute__((ext_vector_type(2)));
typedef float    f32x2  __attribute__((ext_vector_type(2)));
typedef float    f32x4  __attribute__((ext_vector_type(4)));
typedef float    f32x16 __attribute__((ext_vector_type(16)));
typedef unsigned u32x4  __attribute__((ext_vector_type(4)));
typedef unsigned u32x16 __attribute__((ext_vector_type(16)));
typedef unsigned long long u64;
#define LAS __attribute__((address_space(3)))
#define GAS __attribute__((address_space(1)))
#define DI __device__ __forceinline__

#define ROT1 0
#define HELP 1
#define STRICT 0
#define CODEPF 1

constexpr int SEQ = 2048, DM = 512, FF = 1792;
constexpr unsigned UNIT = 16384;
constexpr unsigned H_BYTES = 32u * 14u * 16384u;
constexpr unsigned FLAG_OFF = 8u << 20;
constexpr int CODE_SECT = 160;
constexpr unsigned RED_Q = 64u * 272u;

#define STAMP(i) asm volatile("s_memrealtime %0\n\ts_waitcnt lgkmcnt(0)" : "=s"(ts[i]) :: "memory")

DI void glds2(unsigned ldsaddr, const char* base, unsigned v0, unsigned v1) {
  asm volatile("s_mov_b32 m0, %0\n\ts_nop 0\n\t"
               "global_load_lds_dwordx4 %2, %1\n\t"
               "global_load_lds_dwordx4 %3, %1 offset:1024"
               :: "s"(ldsaddr), "s"(base), "v"(v0), "v"(v1) : "memory");
}
#define WAIT_BAR(n) asm volatile("s_waitcnt vmcnt(" #n ") lgkmcnt(0)\n\ts_barrier" ::: "memory")
#define SCHED __builtin_amdgcn_sched_barrier(0)

DI bf16x8 tr_frag(LAS char* p0, LAS char* p1) {
  s16x4 a = __builtin_amdgcn_ds_read_tr16_b64_v4i16((LAS s16x4*)p0);
  s16x4 b = __builtin_amdgcn_ds_read_tr16_b64_v4i16((LAS s16x4*)p1);
  return __builtin_bit_cast(bf16x8, __builtin_shufflevector(a, b, 0, 1, 2, 3, 4, 5, 6, 7));
}
DI float mish(float h) {
  float E = __builtin_amdgcn_fmed3f(__builtin_amdgcn_exp2f(__builtin_fmaf(h, 1.44269504089f, -14.f)), 0.f, 1.f);
  float N = E * (E + 0x1p-13f);
  return h * (N * __builtin_amdgcn_rcpf(N + 0x1p-27f));
}
DI unsigned pk2(float a, float b) { return __builtin_bit_cast(unsigned, __builtin_convertvector((f32x2){a, b}, bf16x2)); }

__global__ __launch_bounds__(512, 2) void mlp_fused(const char* __restrict__ x, const char* __restrict__ w1,
                                                    const unsigned* __restrict__ b1, const char* __restrict__ w2,
                                                    char* __restrict__ out, char* ws) {
  __shared__ __attribute__((aligned(1024))) char smem[163840];
  LAS char* sm = (LAS char*)smem;
  const unsigned lds0 = (unsigned)(size_t)sm;
  u64 ts[16];
  for (int k = 0; k < 16; k++) ts[k] = 0;
  STAMP(0);
  asm volatile("" :: "s"(x), "s"(w1), "s"(b1), "s"(w2), "s"(out), "s"(ws));
  const int b = blockIdx.x, xcd = b & 7, i = b >> 3;
  const int w = __builtin_amdgcn_readfirstlane(threadIdx.x >> 6), l = threadIdx.x & 63;
  const int r2 = xcd * 4 + (i >> 3), c2 = i & 7;
  const int h5 = l >> 5, blk = (l >> 4) & 1, q4 = (l & 15) >> 2, p = l & 3;
  GAS u64* flags = (GAS u64*)(ws + FLAG_OFF);

  u64 epoch = 0;
  if (w == 0) asm volatile("s_mov_b32 m0, %2\n\ts_nop 0\n\tglobal_load_lds_dword %0, %1 sc1" :: "v"((unsigned)(l & 1) * 4), "s"(ws + FLAG_OFF + 256 * 128), "s"(lds0 + 9 * UNIT) : "memory");
  auto get_epoch = [&]() { const u64 e = *(volatile LAS u64*)(sm + 9 * UNIT);
    epoch = ((u64)(unsigned)__builtin_amdgcn_readfirstlane((unsigned)(e >> 32)) << 32) | (unsigned)__builtin_amdgcn_readfirstlane((unsigned)e); };
  if (w == 1) {
    u64 pc; asm volatile("s_getpc_b64 %0" : "=s"(pc));
    asm volatile("s_mov_b32 m0, %2\n\ts_nop 0\n\tglobal_load_lds_dword %0, %1" :: "v"((unsigned)(min(i * 8 + (l & 7), CODE_SECT - 1) * 64)), "s"(pc), "s"(lds0 + 9 * UNIT + 2048) : "memory");
  }

  const unsigned vw2 = (16 * w + (l >> 3)) * 1024 + (((l & 7) ^ (((l >> 3) & 2) << 1)) * 16);
  const char* w2c = w2 + c2 * 128;
  const unsigned ldsw = lds0 + w * 2048;
  auto stage_w2 = [&](int st, int unit) { glds2(ldsw + unit * UNIT, w2c + st * 131072, vw2, vw2 + 7168); };
  const __amdgpu_buffer_rsrc_t hrs = __builtin_amdgcn_make_buffer_rsrc(ws, 0, H_BYTES, 0x00020000);

  int slot;
  if (__builtin_expect(i < 28, 1)) {
    const int rt = (xcd >> 1) * 4 + (i & 3), ct = (xcd & 1) * 7 + (i >> 2);
    slot = rt * 16 + ct;
    const int rot = ROT1 ? ((i >> 2) + 2 * (i & 3)) : 0;
    const unsigned vx0 = (16 * w + (l >> 3)) * 1024 + (((l & 7) ^ ((l >> 4) & 7)) * 16);
    const unsigned vx1 = (16 * w + 8 + (l >> 3)) * 1024 + (((l & 7) ^ ((4 + (l >> 4)) & 7)) * 16) - 1024;
    const unsigned vw0 = (8 * w + (l >> 4)) * 3584 + (((l & 15) ^ (((l >> 4) << 2) | ((2 * w) & 3))) * 16);
    const unsigned vw1 = (8 * w + 4 + (l >> 4)) * 3584 + (((l & 15) ^ (((l >> 4) << 2) | ((2 * w + 1) & 3))) * 16) - 1024;
    const char* xb = x + rt * 131072;
    const char* wb = w1 + ct * 256;
    auto stage1 = [&](int s) {
      const int ks = (s + rot) & 7;
      glds2(ldsw + (s % 3) * UNIT, xb + ks * 128, vx0, vx1);
      glds2(ldsw + (3 + s % 3) * UNIT, wb + ks * (64 * 3584), vw0, vw1);
    };
    stage1(0); stage1(1); stage1(2);
    STAMP(1);
    const int fq = w & 3, mh = w >> 2;
    f32x16 bias;
    { u32x16 bp;
      asm volatile("s_load_dwordx16 %0, %1, 0\n\ts_waitcnt lgkmcnt(0)" : "=&s"(bp) : "s"(b1 + (ct * 128 + fq * 32) / 2) : "memory");
#pragma unroll
      for (int g = 0; g < 4; g++) {
        unsigned e0 = bp[4 * g], e1 = bp[4 * g + 1], e2 = bp[4 * g + 2], e3 = bp[4 * g + 3];
        asm("" : "+s"(e0), "+s"(e1), "+s"(e2), "+s"(e3));
        const unsigned lo = h5 ? e2 : e0, hi = h5 ? e3 : e1;
        bias[4 * g] = __uint_as_float(lo << 16); bias[4 * g + 1] = __uint_as_float(lo & 0xffff0000u);
        bias[4 * g + 2] = __uint_as_float(hi << 16); bias[4 * g + 3] = __uint_as_float(hi & 0xffff0000u);
      } }
    f32x16 acc0 = bias, acc1 = bias;
    LAS char* xa[4];
#pragma unroll
    for (int ks = 0; ks < 4; ks++) xa[ks] = sm + (64 * mh + (l & 31)) * 128 + (((2 * ks + h5) ^ ((l >> 1) & 7)) * 16);
    LAS char* wa[2];
#pragma unroll
    for (int t = 0; t < 2; t++)
      wa[t] = sm + 3 * UNIT + (8 * h5 + 4 * t + q4) * 256 + (((4 * fq + 2 * blk + (p >> 1)) ^ ((q4 << 2) | ((2 * h5 + t) & 3))) * 16) + 8 * (p & 1);
#pragma unroll
    for (int s = 0; s < 8; s++) {
      SCHED;
      if (s == 0) { WAIT_BAR(8); } else { WAIT_BAR(4); }
      if (s == 0) { STAMP(2); }
      if (s == 1) get_epoch();
      if (s >= 1 && s <= 5) stage1(s + 2);
      if (s == 6) { stage_w2(0, 6); stage_w2(1, 7); }
      if (s == 7) { stage_w2(2, 8); stage_w2(3, 9); STAMP(3); }
      const unsigned so = (s % 3) * UNIT;
      bf16x8 a[4], b0[4], b1v[4];
#pragma unroll
      for (int ks = 0; ks < 4; ks++) {
        a[ks] = tr_frag(wa[0] + so + ks * 4096, wa[1] + so + ks * 4096);
        b0[ks] = *(LAS bf16x8*)(xa[ks] + so); b1v[ks] = *(LAS bf16x8*)(xa[ks] + so + 4096);
        SCHED;
      }
#pragma unroll
      for (int ks = 0; ks < 4; ks++) {
        acc0 = __builtin_amdgcn_mfma_f32_32x32x16_bf16(a[ks], b0[ks], acc0, 0, 0, 0);
        acc1 = __builtin_amdgcn_mfma_f32_32x32x16_bf16(a[ks], b1v[ks], acc1, 0, 0, 0);
      }
    }
    SCHED;
    STAMP(4);
    { const unsigned hv = l * 16 + (((rt * 2 + mh) * 14 + ct) * 4 + fq) * 4096;
#pragma unroll
      for (int s = 0; s < 2; s++) {
        u32x4 o0, o1;
#pragma unroll
        for (int j = 0; j < 4; j++) {
          o0[j] = pk2(mish(acc0[8 * s + 2 * j]), mish(acc0[8 * s + 2 * j + 1]));
          o1[j] = pk2(mish(acc1[8 * s + 2 * j]), mish(acc1[8 * s + 2 * j + 1]));
        }
        __builtin_amdgcn_raw_buffer_store_b128(o0, hrs, hv + s * 1024, 0, 16);
        __builtin_amdgcn_raw_buffer_store_b128(o1, hrs, hv + 2048 + s * 1024, 0, 16);
        SCHED;
      } }
    STAMP(5);
    stage_w2(4, 2); stage_w2(5, 5); stage_w2(6, 0); stage_w2(7, 3);
    WAIT_BAR(8);
    STAMP(6);
    if (w == 0) { if (l == 0) __hip_atomic_store(flags + slot * 16, epoch + 1, __ATOMIC_RELAXED, __HIP_MEMORY_SCOPE_AGENT); }
    stage_w2(8, 1); stage_w2(9, 4);
  } else {
    const int hid = xcd * 4 + (i - 28);
    slot = (hid >> 1) * 16 + 14 + (hid & 1);
    const char* w2o = w2 + (c2 - 4) * 128;
    constexpr int UN[10] = {6, 7, 8, 9, 2, 5, 0, 3, 1, 4};
#pragma unroll
    for (int st = 0; st < 10; st++) {
      glds2(ldsw + UN[st] * UNIT, w2o + st * 131072, vw2, vw2 + 7168);
      stage_w2(st, UN[st]);
      if (st == 2) {
        asm volatile("s_waitcnt vmcnt(%0)" :: "i"(HELP ? 12 : 6) : "memory");
        get_epoch();
        if (w == 0 && l == 0) __hip_atomic_store(flags + slot * 16, epoch + 1, __ATOMIC_RELAXED, __HIP_MEMORY_SCOPE_AGENT);
      }
    }
  }

  if (w == 0) {
    const u64 tag = epoch + 1;
    const int nk = b == 255 ? 4 : 1;
    GAS u64* fp = flags + (b == 255 ? l : (r2 >> 1) * 16 + (l < 14 ? l : 0)) * 16;
#pragma nounroll
    for (int k = 0; k < nk; k++) {
#pragma nounroll
      for (int spins = 0; spins < (1 << 18); spins++) {
        if (__all(__hip_atomic_load(fp + k * 1024, __ATOMIC_RELAXED, __HIP_MEMORY_SCOPE_AGENT) == tag)) break;
        __builtin_amdgcn_s_sleep(1);
      }
    }
    if (b == 255 && l == 0) __hip_atomic_store(flags + 256 * 16, tag, __ATOMIC_RELAXED, __HIP_MEMORY_SCOPE_AGENT);
  }
  SCHED;
  asm volatile("s_barrier" ::: "memory");
  STAMP(7);

  {
    const int q = w & 3, mb = w >> 2;
    constexpr int DEP = 6;
    const unsigned hv = l * 16 + (q * 2 + mb) * 2048;
    const unsigned hs = r2 * 14 * UNIT;
    u32x4 hf[14][2];
    auto load_h = [&](int st) {
      hf[st][0] = __builtin_amdgcn_raw_buffer_load_b128(hrs, hv, hs + st * UNIT, 16);
      hf[st][1] = __builtin_amdgcn_raw_buffer_load_b128(hrs, hv + 1024, hs + st * UNIT, 16);
    };
#pragma unroll
    for (int st = 0; st < DEP; st++) load_h(st);
    SCHED;
    WAIT_BAR(12);
    STAMP(8);
    LAS char* wa[2];
#pragma unroll
    for (int nb = 0; nb < 2; nb++)
      wa[nb] = sm + (32 * q + 4 * h5 + q4) * 128 + (((4 * nb + 2 * blk + (p >> 1)) ^ ((q4 & 2) << 1)) * 16) + 8 * (p & 1);
    f32x16 acc0 = {}, acc1 = {};
    constexpr int UN[14] = {6, 7, 8, 9, 2, 5, 0, 3, 1, 4, 6, 7, 8, 9};
#pragma unroll
    for (int st = 0; st < 14; st++) {
      SCHED;
      if (st == 4) { asm volatile("s_waitcnt lgkmcnt(0)\n\ts_barrier" ::: "memory");
        stage_w2(10, 6); stage_w2(11, 7); stage_w2(12, 8); stage_w2(13, 9); }
      if (st == 10) { WAIT_BAR(8); STAMP(9); }
      const unsigned uo = UN[st] * UNIT;
      bf16x8 a0[2], a1[2];
#pragma unroll
      for (int s = 0; s < 2; s++) {
        a0[s] = tr_frag(wa[0] + uo + s * 2048, wa[0] + uo + s * 2048 + 1024);
        a1[s] = tr_frag(wa[1] + uo + s * 2048, wa[1] + uo + s * 2048 + 1024);
        SCHED;
      }
#pragma unroll
      for (int s = 0; s < 2; s++) {
        const bf16x8 hb = __builtin_bit_cast(bf16x8, hf[st][s]);
        acc0 = __builtin_amdgcn_mfma_f32_32x32x16_bf16(a0[s], hb, acc0, 0, 0, 0);
        acc1 = __builtin_amdgcn_mfma_f32_32x32x16_bf16(a1[s], hb, acc1, 0, 0, 0);
      }
      if (st + DEP < 14) load_h(st + DEP);
    }
    SCHED;
    STAMP(10);
    LAS char* rw = sm + q * RED_Q + (32 * mb + (l & 31)) * 272 + h5 * 16;
#pragma unroll
    for (int g = 0; g < 4; g++) {
      *(LAS f32x4*)(rw + g * 32) = (f32x4){acc0[4 * g], acc0[4 * g + 1], acc0[4 * g + 2], acc0[4 * g + 3]};
      *(LAS f32x4*)(rw + 128 + g * 32) = (f32x4){acc1[4 * g], acc1[4 * g + 1], acc1[4 * g + 2], acc1[4 * g + 3]};
    }
    asm volatile("s_waitcnt lgkmcnt(0)\n\ts_barrier" ::: "memory");
    LAS char* rr = sm + (8 * w + (l >> 3)) * 272 + (l & 7) * 32;
    f32x4 s0 = *(LAS f32x4*)rr, s1 = *(LAS f32x4*)(rr + 16);
#pragma unroll
    for (int k = 1; k < 4; k++) { s0 += *(LAS f32x4*)(rr + k * RED_Q); s1 += *(LAS f32x4*)(rr + k * RED_Q + 16); }
    const u32x4 o = {pk2(s0[0], s0[1]), pk2(s0[2], s0[3]), pk2(s1[0], s1[1]), pk2(s1[2], s1[3])};
    *(u32x4*)(out + (size_t)(r2 * 64 + 8 * w + (l >> 3)) * 1024 + c2 * 128 + (l & 7) * 16) = o;
  }
  STAMP(11);
  asm volatile("s_waitcnt vmcnt(0)" ::: "memory");
  STAMP(12);
  if (b == AMPB && w == 0) { const u64 tgt = ts[0] + 3000 + 8 * (ts[AMP] - ts[0]); u64 t1; do { asm volatile("s_memrealtime %0\n\ts_waitcnt lgkmcnt(0)" : "=s"(t1) :: "memory"); } while (t1 < tgt); }
}

extern "C" void kernel_launch(void* const* d_in, const int* in_sizes, int n_in, void* d_out, int out_size,
                              void* d_ws, size_t ws_size, hipStream_t stream) {
  mlp_fused<<<256, 512, 0, stream>>>((const char*)d_in[0], (const char*)d_in[1], (const unsigned*)d_in[2],
                                     (const char*)d_in[3], (char*)d_out, (char*)d_ws);
}
```

```cpp
#include <hip/hip_runtime.h>
#include <stdint.h>

typedef short    s16x4  __attribute__((ext_vector_type(4)));
typedef __bf16   bf16x8 __attribute__((ext_vector_type(8)));
typedef __bf16   bf16x2 __attribute__((ext_vector_type(2)));
typedef float    f32x2  __attribute__((ext_vector_type(2)));
typedef float    f32x4  __attribute__((ext_vector_type(4)));
typedef unsigned u32x4  __attribute__((ext_vector_type(4)));
typedef unsigned u32x2  __attribute__((ext_vector_type(2)));
typedef unsigned u32x16 __attribute__((ext_vector_type(16)));
typedef unsigned long long u64;
#define LAS __attribute__((address_space(3)))
#define GAS __attribute__((address_space(1)))
#define DI __device__ __forceinline__

#define HELP 2
#define CODEPF 1
#define DEP 4
#define PSLEEP 0
#define THR -1
#define LPRIO 0
#define POLLW 1
#define HPF 1
#define PRE 8

constexpr unsigned UNIT = 16384;
constexpr unsigned H_BYTES = 32u * 14u * 16384u;
constexpr unsigned FLAG_OFF = 8u << 20;
constexpr int CODE_SECT = 192;
constexpr unsigned RED_Q = 64u * 272u;

#define STAMP(i)

DI void glds4(unsigned ldsaddr, const char* base, const unsigned (&v)[4]) {
  asm volatile("s_mov_b32 m0, %0\n\ts_nop 0\n\t"
               "global_load_lds_dwordx4 %2, %1\n\t"
               "global_load_lds_dwordx4 %3, %1 offset:1024\n\t"
               "global_load_lds_dwordx4 %4, %1 offset:2048\n\t"
               "global_load_lds_dwordx4 %5, %1 offset:3072"
               :: "s"(__builtin_amdgcn_readfirstlane(ldsaddr)), "s"(base), "v"(v[0]), "v"(v[1]), "v"(v[2]), "v"(v[3]) : "memory");
}
#define VM_BAR(n) asm volatile("s_waitcnt vmcnt(%0)\n\ts_barrier" :: "i"(n) : "memory")
#define LG_BAR do { __builtin_amdgcn_s_waitcnt(0xC07F); asm volatile("s_barrier" ::: "memory"); } while (0)
#define BAR asm volatile("s_barrier" ::: "memory")
#define SCHED __builtin_amdgcn_sched_barrier(0)

DI bf16x8 tr_frag(LAS char* p0, LAS char* p1) {
  s16x4 a = __builtin_amdgcn_ds_read_tr16_b64_v4i16((LAS s16x4*)p0);
  s16x4 b = __builtin_amdgcn_ds_read_tr16_b64_v4i16((LAS s16x4*)p1);
  return __builtin_bit_cast(bf16x8, __builtin_shufflevector(a, b, 0, 1, 2, 3, 4, 5, 6, 7));
}
DI unsigned pk2(f32x2 v) { return __builtin_bit_cast(unsigned, __builtin_convertvector(v, bf16x2)); }
DI unsigned mish2(float h0, float h1) {
  const f32x2 h = {h0, h1};
  const f32x2 t = h * 1.44269504089f + (-14.f);
  const f32x2 E = {__builtin_amdgcn_fmed3f(__builtin_amdgcn_exp2f(t.x), 0.f, 1.f), __builtin_amdgcn_fmed3f(__builtin_amdgcn_exp2f(t.y), 0.f, 1.f)};
  const f32x2 W = E + 0x1p-13f;
  const f32x2 D = E * W + 0x1p-27f;
  const f32x2 R = {__builtin_amdgcn_rcpf(D.x), __builtin_amdgcn_rcpf(D.y)};
  return pk2(h * (E * (W * R)));
}
#define MFMA(a, b, c) __builtin_amdgcn_mfma_f32_16x16x32_bf16(a, b, c, 0, 0, 0)

DI constexpr int w2unit(int st) { constexpr int U[14] = {6, 7, 8, 9, 2, 5, 0, 3, 1, 4, 6, 7, 8, 9}; return U[st]; }

__global__ __launch_bounds__(768, 3) void mlp_fused(const char* __restrict__ x, const char* __restrict__ w1,
                                                    const unsigned* __restrict__ b1, const char* __restrict__ w2,
                                                    char* __restrict__ out, char* ws) {
  __shared__ __attribute__((aligned(1024))) char smem[163840];
  LAS char* sm = (LAS char*)smem;
  const unsigned lds0 = (unsigned)(size_t)sm;
  STAMP(0);
  asm volatile("" :: "s"(x), "s"(w1), "s"(b1), "s"(w2), "s"(out), "s"(ws));
  const int b = blockIdx.x, xcd = b & 7, i = b >> 3;
  const int w = __builtin_amdgcn_readfirstlane(threadIdx.x >> 6), l = threadIdx.x & 63;
  const int c2 = i & 7;
  const int rt = (xcd >> 1) * 4 + (i & 3), ct = (xcd & 1) * 7 + (i >> 2);

  if (__builtin_expect(w >= 8, 1)) {
    __builtin_amdgcn_s_setprio(LPRIO);
    const int j = w - 8;
    const unsigned ldsw = lds0 + j * 4096;
    unsigned vw2[4];
    const char* w2c = w2 + c2 * 128;
    auto stage_w2 = [&](int st) { glds4(ldsw + w2unit(st) * UNIT, w2c + st * 131072, vw2); };
    auto thr = [&]() { if (THR >= 0) asm volatile("s_waitcnt vmcnt(%0)" :: "i"(THR >= 0 ? THR : 0) : "memory"); };
    if (__builtin_expect(i < 28, 1)) {
      unsigned vx[4], vw[4];
      vx[0] = (32 * j + (l >> 3)) * 1024 + (((l & 7) ^ (l >> 4)) * 16);
      vw[0] = (16 * j + (l >> 4)) * 3584 + (((l & 15) ^ ((l >> 4) << 2)) * 16);
#pragma unroll
      for (int k = 1; k < 4; k++) { vx[k] = (vx[0] ^ ((k & 1) * 64)) + k * 7168; vw[k] = (vw[0] ^ (k * 16)) + k * (4 * 3584 - 1024); }
      const char* xb = x + rt * 131072;
      const char* wb = w1 + ct * 256;
      auto stage1 = [&](int s) {
        glds4(ldsw + (s % 3) * UNIT, xb + s * 128, vx);
        glds4(ldsw + (3 + s % 3) * UNIT, wb + s * (64 * 3584), vw);
      };
      stage1(0);
      if (j == 0) asm volatile("s_mov_b32 m0, %2\n\ts_mov_b64 exec, 3\n\tglobal_load_lds_dword %0, %1 sc1\n\ts_mov_b64 exec, -1"
                               :: "v"(l * 4), "s"(ws + FLAG_OFF + 256 * 128), "s"(lds0 + 9 * UNIT) : "memory");
      if (j == 1) { u64 pc; asm volatile("s_getpc_b64 %0" : "=s"(pc));
        asm volatile("s_mov_b32 m0, %2\n\ts_mov_b64 exec, 0xff\n\tglobal_load_lds_dword %0, %1\n\ts_mov_b64 exec, -1"
                     :: "v"((unsigned)min(i * 7 + l, CODE_SECT - 1) * 64), "s"(pc), "s"(lds0 + 9 * UNIT + 4096) : "memory"); }
      BAR;
      stage1(1);
#pragma unroll
      for (int k = 0; k < 4; k++) vw2[k] = (32 * j + (l >> 3)) * 1024 + (((l & 7) ^ (((l >> 4) & 3) << 1)) * 16) + k * 7168;
#pragma unroll
      for (int s = 0; s < 8; s++) {
        VM_BAR(8);
        if (s < 6) stage1(s + 2);
        if (s == 6) { stage_w2(0); stage_w2(1); }
        if (s == 0) STAMP(2);
      }
#pragma unroll
      for (int st = 2; st < PRE; st++) stage_w2(st);
      STAMP(3);
      VM_BAR(4 * (PRE - 4));
      STAMP(5);
      BAR;
      STAMP(7);
#pragma unroll
      for (int st = PRE; st < 14; st++) { stage_w2(st); thr(); }
      STAMP(8);
    } else {
#pragma unroll
      for (int k = 0; k < 4; k++) vw2[k] = (32 * j + (l >> 3)) * 1024 + (((l & 7) ^ (((l >> 4) & 3) << 1)) * 16) + k * 7168;
#pragma nounroll
      for (int st = 0; st < 14; st++) {
        if (st == 4) VM_BAR(0);
        if (st == 10) BAR;
        const int u = (int)(0x98764130529876ull >> (4 * st)) & 15;
        if (HELP > 1 || (HELP == 1 && st < 10)) glds4(ldsw + u * UNIT, w2c - 512 + st * 131072, vw2);
        glds4(ldsw + u * UNIT, w2c + st * 131072, vw2);
        asm volatile("s_waitcnt vmcnt(16)" ::: "memory");
      }
    }
    BAR;
    VM_BAR(0);
    STAMP(12);
    if (b == 255 && j == 0) {
      GAS u64* flags = (GAS u64*)(ws + FLAG_OFF);
      const u64 tag = __hip_atomic_load(flags + 256 * 16, __ATOMIC_RELAXED, __HIP_MEMORY_SCOPE_AGENT) + 1;
#pragma nounroll
      for (int spins = 0; spins < (1 << 18); spins++) {
        u64 v[4];
#pragma unroll
        for (int k = 0; k < 4; k++) v[k] = __hip_atomic_load(flags + (k * 64 + l) * 16, __ATOMIC_RELAXED, __HIP_MEMORY_SCOPE_AGENT);
        if (__all(v[0] == tag && v[1] == tag && v[2] == tag && v[3] == tag)) break;
      }
      if (l == 0) __hip_atomic_store(flags + 256 * 16, tag, __ATOMIC_RELAXED, __HIP_MEMORY_SCOPE_AGENT);
    }
    return;
  }

  if (i < 28) BAR;
  STAMP(1);
  const int g = l >> 4, q4 = (l & 15) >> 2, p = l & 3;
  GAS u64* flags = (GAS u64*)(ws + FLAG_OFF);
  const __amdgpu_buffer_rsrc_t hrs = __builtin_amdgcn_make_buffer_rsrc(ws, 0, H_BYTES, 0x00020000);
  u64 tag;

  if (__builtin_expect(i < 28, 1)) {
    const int fq = w & 3, mh = w >> 2;
    f32x4 acc[2][4];
    { u32x16 bp;
      asm volatile("s_load_dwordx16 %0, %1, 0\n\ts_waitcnt lgkmcnt(0)" : "=&s"(bp) : "s"(b1 + (ct * 128 + fq * 32) / 2) : "memory");
#pragma unroll
      for (int fb = 0; fb < 2; fb++) {
        unsigned bb[2];
#pragma unroll
        for (int k = 0; k < 2; k++) {
          unsigned e0 = bp[8 * fb + k], e1 = bp[8 * fb + 2 + k], e2 = bp[8 * fb + 4 + k], e3 = bp[8 * fb + 6 + k];
          asm("" : "+s"(e0), "+s"(e1), "+s"(e2), "+s"(e3));
          bb[k] = (g & 2) ? ((g & 1) ? e3 : e2) : ((g & 1) ? e1 : e0);
        }
        const f32x4 bv = {__uint_as_float(bb[0] << 16), __uint_as_float(bb[0] & 0xffff0000u), __uint_as_float(bb[1] << 16), __uint_as_float(bb[1] & 0xffff0000u)};
#pragma unroll
        for (int m = 0; m < 4; m++) acc[fb][m] = bv;
      } }
    LAS char* xa[2];
#pragma unroll
    for (int ks = 0; ks < 2; ks++) xa[ks] = sm + (64 * mh + (l & 15)) * 128 + (((4 * ks + g) ^ ((l >> 1) & 7)) * 16);
    LAS char* wa[2][2];
#pragma unroll
    for (int fb = 0; fb < 2; fb++)
#pragma unroll
      for (int t = 0; t < 2; t++) {
        unsigned o = 3 * UNIT + (8 * g + 4 * t + q4) * 256 + (((4 * fq + 2 * fb + (p >> 1)) ^ ((q4 << 2) | ((2 * g + t) & 3))) * 16) + 8 * (p & 1);
        asm("" : "+v"(o));
        wa[fb][t] = sm + o;
      }
    bf16x8 a[2][2], bx[2][4];
    bf16x8 pa[2], pb[2];
    const unsigned hv = l * 16 + (((rt * 2 + mh) * 14 + ct) * 4 + fq) * 4096;
#pragma unroll
    for (int s = 0; s < 8; s++) {
      SCHED;
      LG_BAR;
      if (s == 0) STAMP(2);
      if (s == 7) STAMP(3);
      if (s == 1) tag = *(volatile LAS u64*)(sm + 9 * UNIT) + 1;
      const unsigned so = (s % 3) * UNIT;
      auto rda = [&](int ks, int fb) { a[ks][fb] = tr_frag(wa[fb][0] + so + ks * 8192, wa[fb][1] + so + ks * 8192); SCHED; };
      auto rdb = [&](int ks, int m) { bx[ks][m] = *(LAS bf16x8*)(xa[ks] + so + m * 2048); bx[ks][m + 1] = *(LAS bf16x8*)(xa[ks] + so + m * 2048 + 2048); SCHED; };
      auto mm = [&](int ks, int fb, int m) { acc[fb][m] = MFMA(a[ks][fb], bx[ks][m], acc[fb][m]); SCHED; };
      auto late = [&](int fb, int m) { if (s > 0) { acc[fb][2 + m] = MFMA(pa[fb], pb[m], acc[fb][2 + m]); SCHED; } };
      late(0, 0); rda(0, 0); late(1, 0); rdb(0, 0); late(0, 1); rda(0, 1); late(1, 1); rdb(0, 2);
      if (s < 7) {
        mm(0, 0, 0); rda(1, 0); mm(0, 0, 1); rdb(1, 0); mm(0, 1, 0); rda(1, 1); mm(0, 1, 1); rdb(1, 2);
        mm(0, 0, 2); mm(0, 0, 3); mm(0, 1, 2); mm(0, 1, 3);
        mm(1, 0, 0); mm(1, 0, 1); mm(1, 1, 0); mm(1, 1, 1);
        pa[0] = a[1][0]; pa[1] = a[1][1]; pb[0] = bx[1][2]; pb[1] = bx[1][3];
      } else {
        mm(0, 0, 0); rda(1, 0); mm(0, 1, 0); rdb(1, 0); mm(0, 0, 1); rda(1, 1); mm(0, 1, 1); rdb(1, 2);
        mm(1, 0, 0); mm(1, 1, 0); mm(1, 0, 1); mm(1, 1, 1);
      }
    }
    SCHED;
    STAMP(4);
#pragma unroll
    for (int m = 0; m < 4; m++) {
      if (m < 2) {
#pragma unroll
        for (int ks = 0; ks < 2; ks++)
#pragma unroll
          for (int fb = 0; fb < 2; fb++) acc[fb][2 + m] = MFMA(a[ks][fb], bx[ks][2 + m], acc[fb][2 + m]);
      }
      const u32x4 o = {mish2(acc[0][m][0], acc[0][m][1]), mish2(acc[0][m][2], acc[0][m][3]), mish2(acc[1][m][0], acc[1][m][1]), mish2(acc[1][m][2], acc[1][m][3])};
      __builtin_amdgcn_raw_buffer_store_b128(o, hrs, hv + m * 1024, 0, 16);
      SCHED;
    }
    STAMP(5);
  } else {
    tag = __hip_atomic_load(flags + 256 * 16, __ATOMIC_RELAXED, __HIP_MEMORY_SCOPE_AGENT) + 1;
    const int hid = xcd * 4 + (i - 28);
    if (w == 0 && l == 0) __hip_atomic_store(flags + ((hid >> 1) * 16 + 14 + (hid & 1)) * 16, tag, __ATOMIC_RELAXED, __HIP_MEMORY_SCOPE_AGENT);
  }
  SCHED;
  BAR;
  const int r2 = xcd * 4 + (i >> 3);
  {
    const int q = w & 3, mb = w >> 2;
    LAS char* wa[3][4];
#pragma unroll
    for (int k = 0; k < 3; k++)
#pragma unroll
      for (int c = 0; c < 4; c++) {
        unsigned o = k * 65536 + (32 * q + 4 * g + q4) * 128 + (((2 * c + (p >> 1)) ^ ((((2 * g) | (q4 >> 1)) & 3) << 1)) * 16) + 8 * (p & 1);
        asm("" : "+v"(o));
        wa[k][c] = sm + o;
      }
    bf16x8 af[14][4];
    auto read_w = [&](int st, int c) {
      const int u = w2unit(st);
      LAS char* a = wa[u >> 2][c] + (u & 3) * UNIT;
      af[st][c] = tr_frag(a, a + 2048);
    };
#pragma unroll
    for (int st = 0; st < 4; st++)
#pragma unroll
      for (int c = 0; c < 4; c++) read_w(st, c);
    SCHED;
    asm volatile("s_waitcnt vmcnt(0)" ::: "memory");
    STAMP(6);
    LG_BAR;
    if (w == 0 && i < 28 && l == 0) __hip_atomic_store(flags + (rt * 16 + ct) * 16, tag, __ATOMIC_RELAXED, __HIP_MEMORY_SCOPE_AGENT);
    STAMP(15);
    if (w == POLLW) {
      GAS u64* fp = flags + ((r2 >> 1) * 16 + (l < 14 ? l : 0)) * 16;
#pragma nounroll
      for (int spins = 0; spins < (1 << 18); spins++) {
        if (__all(__hip_atomic_load(fp, __ATOMIC_RELAXED, __HIP_MEMORY_SCOPE_AGENT) == tag)) break;
      }
      STAMP(14);
    }
    if (w == 2) {
      GAS u64* fp = flags + ((r2 >> 1) * 16 + (l < 14 ? l : 0)) * 16;
      unsigned done = 0;
      u32x4 d0 = {}, d1 = {};
#pragma nounroll
      for (int spins = 0; spins < (1 << 18); spins++) {
        const unsigned rdy = (unsigned)__ballot(__hip_atomic_load(fp, __ATOMIC_RELAXED, __HIP_MEMORY_SCOPE_AGENT) == tag) & 0x3fff;
        asm volatile("" :: "v"(d0), "v"(d1));
        if (rdy == 0x3fff) break;
        unsigned nw = rdy & ~done; done = rdy;
#pragma nounroll
        while (nw) {
          const int k = __builtin_ctz(nw); nw &= nw - 1;
          const unsigned so = (r2 * 14 + k) * UNIT + c2 * 2048;
          d0 = __builtin_amdgcn_raw_buffer_load_b128(hrs, l * 16, so, 16);
          d1 = __builtin_amdgcn_raw_buffer_load_b128(hrs, l * 16 + 1024, so, 16);
        }
      }
    }
    SCHED;
    BAR;
    STAMP(7);
    const unsigned hv = l * 16 + (q * 2 + mb) * 2048;
    const unsigned hs = r2 * 14 * UNIT;
    u32x4 hf[14][2];
    auto load_h = [&](int st) {
      hf[st][0] = __builtin_amdgcn_raw_buffer_load_b128(hrs, hv, hs + st * UNIT, 16);
      hf[st][1] = __builtin_amdgcn_raw_buffer_load_b128(hrs, hv + 1024, hs + st * UNIT, 16);
    };
#pragma unroll
    for (int st = 0; st < DEP; st++) load_h(st);
    SCHED;
    STAMP(8);
    f32x4 acc[4][2] = {};
#pragma unroll
    for (int st = 0; st < 14; st++) {
      SCHED;
      if (st == 3) { BAR; STAMP(13); }
      if (st == 9) STAMP(9);
#pragma unroll
      for (int m = 0; m < 2; m++)
#pragma unroll
        for (int c = 0; c < 4; c++) {
          acc[c][m] = MFMA(af[st][c], __builtin_bit_cast(bf16x8, hf[st][m]), acc[c][m]);
          SCHED;
          if (st >= 3 && st + 1 < 14 && m == 0) { read_w(st + 1, c); SCHED; }
        }
      if (st + DEP < 14) load_h(st + DEP);
    }
    SCHED;
    STAMP(10);
    BAR;
    LAS char* rw = sm + q * RED_Q + (32 * mb + (l & 15)) * 272 + g * 16;
#pragma unroll
    for (int m = 0; m < 2; m++)
#pragma unroll
      for (int c = 0; c < 4; c++) *(LAS f32x4*)(rw + m * (16 * 272) + c * 64) = acc[c][m];
    LG_BAR;
    LAS char* rr = sm + (8 * w + (l >> 3)) * 272 + (l & 7) * 32;
    f32x4 s0 = *(LAS f32x4*)rr, s1 = *(LAS f32x4*)(rr + 16);
#pragma unroll
    for (int k = 1; k < 4; k++) { s0 += *(LAS f32x4*)(rr + k * RED_Q); s1 += *(LAS f32x4*)(rr + k * RED_Q + 16); }
    const u32x4 o = {pk2((f32x2){s0[0], s0[1]}), pk2((f32x2){s0[2], s0[3]}), pk2((f32x2){s1[0], s1[1]}), pk2((f32x2){s1[2], s1[3]})};
    *(u32x4*)(out + (size_t)(r2 * 64 + 8 * w + (l >> 3)) * 1024 + c2 * 128 + (l & 7) * 16) = o;
  }
}

extern "C" void kernel_launch(void* const* d_in, const int* in_sizes, int n_in, void* d_out, int out_size,
                              void* d_ws, size_t ws_size, hipStream_t stream) {
  mlp_fused<<<256, 768, 0, stream>>>((const char*)d_in[0], (const char*)d_in[1], (const unsigned*)d_in[2],
                                     (const char*)d_in[3], (char*)d_out, (char*)d_ws);
}
```

```cpp
#define WSOFF ((size_t)128 << 20)
#include <hip/hip_runtime.h>
#include <stdint.h>

typedef short    s16x4  __attribute__((ext_vector_type(4)));
typedef __bf16   bf16x8 __attribute__((ext_vector_type(8)));
typedef __bf16   bf16x2 __attribute__((ext_vector_type(2)));
typedef float    f32x2  __attribute__((ext_vector_type(2)));
typedef float    f32x4  __attribute__((ext_vector_type(4)));
typedef unsigned u32x4  __attribute__((ext_vector_type(4)));
typedef unsigned u32x2  __attribute__((ext_vector_type(2)));
typedef unsigned u32x16 __attribute__((ext_vector_type(16)));
typedef unsigned long long u64;
#define LAS __attribute__((address_space(3)))
#define GAS __attribute__((address_space(1)))
#define DI __device__ __forceinline__

#define AMPW 0
#define AMPSEL true
#define HELP 2
#define CODEPF 1
#define DEP 4
#define PSLEEP 0
#define THR -1
#define LPRIO 0
#define POLLW 1
#define HPF 1
#define PRE 8

constexpr unsigned UNIT = 16384;
constexpr unsigned H_BYTES = 32u * 14u * 16384u;
constexpr unsigned FLAG_OFF = 8u << 20;
constexpr int CODE_SECT = 192;
constexpr unsigned RED_Q = 64u * 272u;

#define STAMP(i)

DI void glds4(unsigned ldsaddr, const char* base, const unsigned (&v)[4]) {
  asm volatile("s_mov_b32 m0, %0\n\ts_nop 0\n\t"
               "global_load_lds_dwordx4 %2, %1\n\t"
               "global_load_lds_dwordx4 %3, %1 offset:1024\n\t"
               "global_load_lds_dwordx4 %4, %1 offset:2048\n\t"
               "global_load_lds_dwordx4 %5, %1 offset:3072"
               :: "s"(__builtin_amdgcn_readfirstlane(ldsaddr)), "s"(base), "v"(v[0]), "v"(v[1]), "v"(v[2]), "v"(v[3]) : "memory");
}
#define VM_BAR(n) asm volatile("s_waitcnt vmcnt(%0)\n\ts_barrier" :: "i"(n) : "memory")
#define LG_BAR do { __builtin_amdgcn_s_waitcnt(0xC07F); asm volatile("s_barrier" ::: "memory"); } while (0)
#define BAR asm volatile("s_barrier" ::: "memory")
#define SCHED __builtin_amdgcn_sched_barrier(0)

DI bf16x8 tr_frag(LAS char* p0, LAS char* p1) {
  s16x4 a = __builtin_amdgcn_ds_read_tr16_b64_v4i16((LAS s16x4*)p0);
  s16x4 b = __builtin_amdgcn_ds_read_tr16_b64_v4i16((LAS s16x4*)p1);
  return __builtin_bit_cast(bf16x8, __builtin_shufflevector(a, b, 0, 1, 2, 3, 4, 5, 6, 7));
}
DI unsigned pk2(f32x2 v) { return __builtin_bit_cast(unsigned, __builtin_convertvector(v, bf16x2)); }
DI unsigned mish2(float h0, float h1) {
  const f32x2 h = {h0, h1};
  const f32x2 t = h * 1.44269504089f + (-14.f);
  const f32x2 E = {__builtin_amdgcn_fmed3f(__builtin_amdgcn_exp2f(t.x), 0.f, 1.f), __builtin_amdgcn_fmed3f(__builtin_amdgcn_exp2f(t.y), 0.f, 1.f)};
  const f32x2 W = E + 0x1p-13f;
  const f32x2 D = E * W + 0x1p-27f;
  const f32x2 R = {__builtin_amdgcn_rcpf(D.x), __builtin_amdgcn_rcpf(D.y)};
  return pk2(h * (E * (W * R)));
}
#define MFMA(a, b, c) __builtin_amdgcn_mfma_f32_16x16x32_bf16(a, b, c, 0, 0, 0)

DI constexpr int w2unit(int st) { constexpr int U[14] = {6, 7, 8, 9, 2, 5, 0, 3, 1, 4, 6, 7, 8, 9}; return U[st]; }

__global__ __launch_bounds__(768, 3) void mlp_fused(const char* __restrict__ x, const char* __restrict__ w1,
                                                    const unsigned* __restrict__ b1, const char* __restrict__ w2,
                                                    char* __restrict__ out, char* ws) {
  __shared__ __attribute__((aligned(1024))) char smem[163840];
  LAS char* sm = (LAS char*)smem;
  const unsigned lds0 = (unsigned)(size_t)sm;
  STAMP(0);
  asm volatile("" :: "s"(x), "s"(w1), "s"(b1), "s"(w2), "s"(out), "s"(ws));
  const int b = blockIdx.x, xcd = b & 7, i = b >> 3;
  const int w = __builtin_amdgcn_readfirstlane(threadIdx.x >> 6), l = threadIdx.x & 63;
  const int c2 = i & 7;
  const int rt = (xcd >> 1) * 4 + (i & 3), ct = (xcd & 1) * 7 + (i >> 2);

  if (__builtin_expect(w >= 8, 1)) {
    __builtin_amdgcn_s_setprio(LPRIO);
    const int j = w - 8;
    const unsigned ldsw = lds0 + j * 4096;
    unsigned vw2[4];
    const char* w2c = w2 + c2 * 128;
    auto stage_w2 = [&](int st) { glds4(ldsw + w2unit(st) * UNIT, w2c + st * 131072, vw2); };
    auto thr = [&]() { if (THR >= 0) asm volatile("s_waitcnt vmcnt(%0)" :: "i"(THR >= 0 ? THR : 0) : "memory"); };
    if (__builtin_expect(i < 28, 1)) {
      unsigned vx[4], vw[4];
      vx[0] = (32 * j + (l >> 3)) * 1024 + (((l & 7) ^ (l >> 4)) * 16);
      vw[0] = (16 * j + (l >> 4)) * 3584 + (((l & 15) ^ ((l >> 4) << 2)) * 16);
#pragma unroll
      for (int k = 1; k < 4; k++) { vx[k] = (vx[0] ^ ((k & 1) * 64)) + k * 7168; vw[k] = (vw[0] ^ (k * 16)) + k * (4 * 3584 - 1024); }
      const char* xb = x + rt * 131072;
      const char* wb = w1 + ct * 256;
      auto stage1 = [&](int s) {
        glds4(ldsw + (s % 3) * UNIT, xb + s * 128, vx);
        glds4(ldsw + (3 + s % 3) * UNIT, wb + s * (64 * 3584), vw);
      };
      stage1(0);
      if (j == 0) asm volatile("s_mov_b32 m0, %2\n\ts_mov_b64 exec, 3\n\tglobal_load_lds_dword %0, %1 sc1\n\ts_mov_b64 exec, -1"
                               :: "v"(l * 4), "s"(ws + FLAG_OFF + 256 * 128), "s"(lds0 + 9 * UNIT) : "memory");
      if (j == 1) { u64 pc; asm volatile("s_getpc_b64 %0" : "=s"(pc));
        asm volatile("s_mov_b32 m0, %2\n\ts_mov_b64 exec, 0xff\n\tglobal_load_lds_dword %0, %1\n\ts_mov_b64 exec, -1"
                     :: "v"((unsigned)min(i * 7 + l, CODE_SECT - 1) * 64), "s"(pc), "s"(lds0 + 9 * UNIT + 4096) : "memory"); }
      BAR;
      stage1(1);
#pragma unroll
      for (int k = 0; k < 4; k++) vw2[k] = (32 * j + (l >> 3)) * 1024 + (((l & 7) ^ (((l >> 4) & 3) << 1)) * 16) + k * 7168;
#pragma unroll
      for (int s = 0; s < 8; s++) {
        VM_BAR(8);
        if (s < 6) stage1(s + 2);
        if (s == 6) { stage_w2(0); stage_w2(1); }
        if (s == 0) STAMP(2);
      }
#pragma unroll
      for (int st = 2; st < PRE; st++) stage_w2(st);
      STAMP(3);
      VM_BAR(4 * (PRE - 4));
      STAMP(5);
      BAR;
      STAMP(7);
#pragma unroll
      for (int st = PRE; st < 14; st++) { stage_w2(st); thr(); }
      STAMP(8);
    } else {
#pragma unroll
      for (int k = 0; k < 4; k++) vw2[k] = (32 * j + (l >> 3)) * 1024 + (((l & 7) ^ (((l >> 4) & 3) << 1)) * 16) + k * 7168;
#pragma nounroll
      for (int st = 0; st < 14; st++) {
        if (st == 4) VM_BAR(0);
        if (st == 10) BAR;
        const int u = (int)(0x98764130529876ull >> (4 * st)) & 15;
        if (HELP > 1 || (HELP == 1 && st < 10)) glds4(ldsw + u * UNIT, w2c - 512 + st * 131072, vw2);
        glds4(ldsw + u * UNIT, w2c + st * 131072, vw2);
        asm volatile("s_waitcnt vmcnt(16)" ::: "memory");
      }
    }
    BAR;
    VM_BAR(0);
    STAMP(12);
    if (b == 255 && j == 0) {
      GAS u64* flags = (GAS u64*)(ws + FLAG_OFF);
      const u64 tag = __hip_atomic_load(flags + 256 * 16, __ATOMIC_RELAXED, __HIP_MEMORY_SCOPE_AGENT) + 1;
#pragma nounroll
      for (int spins = 0; spins < (1 << 18); spins++) {
        u64 v[4];
#pragma unroll
        for (int k = 0; k < 4; k++) v[k] = __hip_atomic_load(flags + (k * 64 + l) * 16, __ATOMIC_RELAXED, __HIP_MEMORY_SCOPE_AGENT);
        if (__all(v[0] == tag && v[1] == tag && v[2] == tag && v[3] == tag)) break;
      }
      if (l == 0) __hip_atomic_store(flags + 256 * 16, tag, __ATOMIC_RELAXED, __HIP_MEMORY_SCOPE_AGENT);
    }
    return;
  }

  if (i < 28) BAR;
  STAMP(1);
  const int g = l >> 4, q4 = (l & 15) >> 2, p = l & 3;
  GAS u64* flags = (GAS u64*)(ws + FLAG_OFF);
  const __amdgpu_buffer_rsrc_t hrs = __builtin_amdgcn_make_buffer_rsrc(ws, 0, H_BYTES, 0x00020000);
  u64 tag;

  if (__builtin_expect(i < 28, 1)) {
    const int fq = w & 3, mh = w >> 2;
    f32x4 acc[2][4];
    { u32x16 bp;
      asm volatile("s_load_dwordx16 %0, %1, 0\n\ts_waitcnt lgkmcnt(0)" : "=&s"(bp) : "s"(b1 + (ct * 128 + fq * 32) / 2) : "memory");
#pragma unroll
      for (int fb = 0; fb < 2; fb++) {
        unsigned bb[2];
#pragma unroll
        for (int k = 0; k < 2; k++) {
          unsigned e0 = bp[8 * fb + k], e1 = bp[8 * fb + 2 + k], e2 = bp[8 * fb + 4 + k], e3 = bp[8 * fb + 6 + k];
          asm("" : "+s"(e0), "+s"(e1), "+s"(e2), "+s"(e3));
          bb[k] = (g & 2) ? ((g & 1) ? e3 : e2) : ((g & 1) ? e1 : e0);
        }
        const f32x4 bv = {__uint_as_float(bb[0] << 16), __uint_as_float(bb[0] & 0xffff0000u), __uint_as_float(bb[1] << 16), __uint_as_float(bb[1] & 0xffff0000u)};
#pragma unroll
        for (int m = 0; m < 4; m++) acc[fb][m] = bv;
      } }
    LAS char* xa[2];
#pragma unroll
    for (int ks = 0; ks < 2; ks++) xa[ks] = sm + (64 * mh + (l & 15)) * 128 + (((4 * ks + g) ^ ((l >> 1) & 7)) * 16);
    LAS char* wa[2][2];
#pragma unroll
    for (int fb = 0; fb < 2; fb++)
#pragma unroll
      for (int t = 0; t < 2; t++) {
        unsigned o = 3 * UNIT + (8 * g + 4 * t + q4) * 256 + (((4 * fq + 2 * fb + (p >> 1)) ^ ((q4 << 2) | ((2 * g + t) & 3))) * 16) + 8 * (p & 1);
        asm("" : "+v"(o));
        wa[fb][t] = sm + o;
      }
    bf16x8 a[2][2], bx[2][4];
    bf16x8 pa[2], pb[2];
    const unsigned hv = l * 16 + (((rt * 2 + mh) * 14 + ct) * 4 + fq) * 4096;
#pragma unroll
    for (int s = 0; s < 8; s++) {
      SCHED;
      LG_BAR;
      if (s == 0) STAMP(2);
      if (s == 7) STAMP(3);
      if (s == 1) tag = *(volatile LAS u64*)(sm + 9 * UNIT) + 1;
      const unsigned so = (s % 3) * UNIT;
      auto rda = [&](int ks, int fb) { a[ks][fb] = tr_frag(wa[fb][0] + so + ks * 8192, wa[fb][1] + so + ks * 8192); SCHED; };
      auto rdb = [&](int ks, int m) { bx[ks][m] = *(LAS bf16x8*)(xa[ks] + so + m * 2048); bx[ks][m + 1] = *(LAS bf16x8*)(xa[ks] + so + m * 2048 + 2048); SCHED; };
      auto mm = [&](int ks, int fb, int m) { acc[fb][m] = MFMA(a[ks][fb], bx[ks][m], acc[fb][m]); SCHED; };
      auto late = [&](int fb, int m) { if (s > 0) { acc[fb][2 + m] = MFMA(pa[fb], pb[m], acc[fb][2 + m]); SCHED; } };
      late(0, 0); rda(0, 0); late(1, 0); rdb(0, 0); late(0, 1); rda(0, 1); late(1, 1); rdb(0, 2);
      if (s < 7) {
        mm(0, 0, 0); rda(1, 0); mm(0, 0, 1); rdb(1, 0); mm(0, 1, 0); rda(1, 1); mm(0, 1, 1); rdb(1, 2);
        mm(0, 0, 2); mm(0, 0, 3); mm(0, 1, 2); mm(0, 1, 3);
        mm(1, 0, 0); mm(1, 0, 1); mm(1, 1, 0); mm(1, 1, 1);
        pa[0] = a[1][0]; pa[1] = a[1][1]; pb[0] = bx[1][2]; pb[1] = bx[1][3];
      } else {
        mm(0, 0, 0); rda(1, 0); mm(0, 1, 0); rdb(1, 0); mm(0, 0, 1); rda(1, 1); mm(0, 1, 1); rdb(1, 2);
        mm(1, 0, 0); mm(1, 1, 0); mm(1, 0, 1); mm(1, 1, 1);
      }
    }
    SCHED;
    STAMP(4);
#pragma unroll
    for (int m = 0; m < 4; m++) {
      if (m < 2) {
#pragma unroll
        for (int ks = 0; ks < 2; ks++)
#pragma unroll
          for (int fb = 0; fb < 2; fb++) acc[fb][2 + m] = MFMA(a[ks][fb], bx[ks][2 + m], acc[fb][2 + m]);
      }
      const u32x4 o = {mish2(acc[0][m][0], acc[0][m][1]), mish2(acc[0][m][2], acc[0][m][3]), mish2(acc[1][m][0], acc[1][m][1]), mish2(acc[1][m][2], acc[1][m][3])};
      __builtin_amdgcn_raw_buffer_store_b128(o, hrs, hv + m * 1024, 0, 16);
      SCHED;
    }
    STAMP(5);
  } else {
    tag = __hip_atomic_load(flags + 256 * 16, __ATOMIC_RELAXED, __HIP_MEMORY_SCOPE_AGENT) + 1;
    const int hid = xcd * 4 + (i - 28);
    if (w == 0 && l == 0) __hip_atomic_store(flags + ((hid >> 1) * 16 + 14 + (hid & 1)) * 16, tag, __ATOMIC_RELAXED, __HIP_MEMORY_SCOPE_AGENT);
  }
  SCHED;
  BAR;
  const int r2 = xcd * 4 + (i >> 3);
  {
    const int q = w & 3, mb = w >> 2;
    LAS char* wa[3][4];
#pragma unroll
    for (int k = 0; k < 3; k++)
#pragma unroll
      for (int c = 0; c < 4; c++) {
        unsigned o = k * 65536 + (32 * q + 4 * g + q4) * 128 + (((2 * c + (p >> 1)) ^ ((((2 * g) | (q4 >> 1)) & 3) << 1)) * 16) + 8 * (p & 1);
        asm("" : "+v"(o));
        wa[k][c] = sm + o;
      }
    bf16x8 af[14][4];
    auto read_w = [&](int st, int c) {
      const int u = w2unit(st);
      LAS char* a = wa[u >> 2][c] + (u & 3) * UNIT;
      af[st][c] = tr_frag(a, a + 2048);
    };
#pragma unroll
    for (int st = 0; st < 4; st++)
#pragma unroll
      for (int c = 0; c < 4; c++) read_w(st, c);
    SCHED;
    asm volatile("s_waitcnt vmcnt(0)" ::: "memory");
    STAMP(6);
    LG_BAR;
    if (w == 0 && i < 28 && l == 0) __hip_atomic_store(flags + (rt * 16 + ct) * 16, tag, __ATOMIC_RELAXED, __HIP_MEMORY_SCOPE_AGENT);
    STAMP(15);
    if (w == POLLW) {
      GAS u64* fp = flags + ((r2 >> 1) * 16 + (l < 14 ? l : 0)) * 16;
#pragma nounroll
      for (int spins = 0; spins < (1 << 18); spins++) {
        if (__all(__hip_atomic_load(fp, __ATOMIC_RELAXED, __HIP_MEMORY_SCOPE_AGENT) == tag)) break;
      }
      STAMP(14);
    }
    if (w == 2) {
      GAS u64* fp = flags + ((r2 >> 1) * 16 + (l < 14 ? l : 0)) * 16;
      unsigned done = 0;
      u32x4 d0 = {}, d1 = {};
#pragma nounroll
      for (int spins = 0; spins < (1 << 18); spins++) {
        const unsigned rdy = (unsigned)__ballot(__hip_atomic_load(fp, __ATOMIC_RELAXED, __HIP_MEMORY_SCOPE_AGENT) == tag) & 0x3fff;
        asm volatile("" :: "v"(d0), "v"(d1));
        if (rdy == 0x3fff) break;
        unsigned nw = rdy & ~done; done = rdy;
#pragma nounroll
        while (nw) {
          const int k = __builtin_ctz(nw); nw &= nw - 1;
          const unsigned so = (r2 * 14 + k) * UNIT + c2 * 2048;
          d0 = __builtin_amdgcn_raw_buffer_load_b128(hrs, l * 16, so, 16);
          d1 = __builtin_amdgcn_raw_buffer_load_b128(hrs, l * 16 + 1024, so, 16);
        }
      }
    }
    SCHED;
    BAR;
    STAMP(7);
    const unsigned hv = l * 16 + (q * 2 + mb) * 2048;
    const unsigned hs = r2 * 14 * UNIT;
    u32x4 hf[14][2];
    auto load_h = [&](int st) {
      hf[st][0] = __builtin_amdgcn_raw_buffer_load_b128(hrs, hv, hs + st * UNIT, 16);
      hf[st][1] = __builtin_amdgcn_raw_buffer_load_b128(hrs, hv + 1024, hs + st * UNIT, 16);
    };
#pragma unroll
    for (int st = 0; st < DEP; st++) load_h(st);
    SCHED;
    STAMP(8);
    f32x4 acc[4][2] = {};
#pragma unroll
    for (int st = 0; st < 14; st++) {
      SCHED;
      if (st == 3) { BAR; STAMP(13); }
      if (st == 9) STAMP(9);
#pragma unroll
      for (int m = 0; m < 2; m++)
#pragma unroll
        for (int c = 0; c < 4; c++) {
          acc[c][m] = MFMA(af[st][c], __builtin_bit_cast(bf16x8, hf[st][m]), acc[c][m]);
          SCHED;
          if (st >= 3 && st + 1 < 14 && m == 0) { read_w(st + 1, c); SCHED; }
        }
      if (st + DEP < 14) load_h(st + DEP);
    }
    SCHED;
    STAMP(10);
    BAR;
    LAS char* rw = sm + q * RED_Q + (32 * mb + (l & 15)) * 272 + g * 16;
#pragma unroll
    for (int m = 0; m < 2; m++)
#pragma unroll
      for (int c = 0; c < 4; c++) *(LAS f32x4*)(rw + m * (16 * 272) + c * 64) = acc[c][m];
    LG_BAR;
    LAS char* rr = sm + (8 * w + (l >> 3)) * 272 + (l & 7) * 32;
    f32x4 s0 = *(LAS f32x4*)rr, s1 = *(LAS f32x4*)(rr + 16);
#pragma unroll
    for (int k = 1; k < 4; k++) { s0 += *(LAS f32x4*)(rr + k * RED_Q); s1 += *(LAS f32x4*)(rr + k * RED_Q + 16); }
    const u32x4 o = {pk2((f32x2){s0[0], s0[1]}), pk2((f32x2){s0[2], s0[3]}), pk2((f32x2){s1[0], s1[1]}), pk2((f32x2){s1[2], s1[3]})};
    *(u32x4*)(out + (size_t)(r2 * 64 + 8 * w + (l >> 3)) * 1024 + c2 * 128 + (l & 7) * 16) = o;
  }
}

extern "C" void kernel_launch(void* const* d_in, const int* in_sizes, int n_in, void* d_out, int out_size,
                              void* d_ws, size_t ws_size, hipStream_t stream) {
  mlp_fused<<<256, 768, 0, stream>>>((const char*)d_in[0], (const char*)d_in[1], (const unsigned*)d_in[2],
                                     (const char*)d_in[3], (char*)d_out, (char*)d_ws + WSOFF);
}
```

```cpp
#define STAMPS
#define AMP 0
#define AMPSEL false
#define PROBE 2097280
#include <hip/hip_runtime.h>
#include <stdint.h>

typedef short    s16x4  __attribute__((ext_vector_type(4)));
typedef __bf16   bf16x8 __attribute__((ext_vector_type(8)));
typedef __bf16   bf16x2 __attribute__((ext_vector_type(2)));
typedef float    f32x2  __attribute__((ext_vector_type(2)));
typedef float    f32x4  __attribute__((ext_vector_type(4)));
typedef unsigned u32x4  __attribute__((ext_vector_type(4)));
typedef unsigned u32x2  __attribute__((ext_vector_type(2)));
typedef unsigned u32x16 __attribute__((ext_vector_type(16)));
typedef unsigned long long u64;
#define LAS __attribute__((address_space(3)))
#define GAS __attribute__((address_space(1)))
#define DI __device__ __forceinline__

#define AMPW 0
#define WSOFF 0
#define HELP 2
#define CODEPF 1
#define DEP 4
#define PSLEEP 0
#define THR -1
#define LPRIO 0
#define POLLW 1
#define HPF 1
#define PRE 8

constexpr unsigned UNIT = 16384;
constexpr unsigned H_BYTES = 32u * 14u * 16384u;
constexpr unsigned FLAG_OFF = 8u << 20;
constexpr int CODE_SECT = 192;
constexpr unsigned RED_Q = 64u * 272u;

#define STAMP_INS "s_memrealtime"
#define STAMP(i) do { u64 t_; asm volatile(STAMP_INS " %0\n\ts_waitcnt lgkmcnt(0)" : "=s"(t_) :: "memory"); \
    asm volatile("v_writelane_b32 %0, %2, %4\n\tv_writelane_b32 %1, %3, %4" : "+v"(tlo), "+v"(thi) : "s"((unsigned)t_), "s"((unsigned)(t_ >> 32)), "i"(i)); } while (0)
#define STAMP_OUT do { if (l < 16) ((u64*)(ws + (16u << 20)))[(b * 16 + w) * 16 + l] = ((u64)thi << 32) | tlo; } while (0)
#define TS(i) (((u64)(unsigned)__builtin_amdgcn_readlane(thi, i) << 32) | (unsigned)__builtin_amdgcn_readlane(tlo, i))

DI void glds4(unsigned ldsaddr, const char* base, const unsigned (&v)[4]) {
  asm volatile("s_mov_b32 m0, %0\n\ts_nop 0\n\t"
               "global_load_lds_dwordx4 %2, %1\n\t"
               "global_load_lds_dwordx4 %3, %1 offset:1024\n\t"
               "global_load_lds_dwordx4 %4, %1 offset:2048\n\t"
               "global_load_lds_dwordx4 %5, %1 offset:3072"
               :: "s"(__builtin_amdgcn_readfirstlane(ldsaddr)), "s"(base), "v"(v[0]), "v"(v[1]), "v"(v[2]), "v"(v[3]) : "memory");
}
#define VM_BAR(n) asm volatile("s_waitcnt vmcnt(%0)\n\ts_barrier" :: "i"(n) : "memory")
#define LG_BAR do { __builtin_amdgcn_s_waitcnt(0xC07F); asm volatile("s_barrier" ::: "memory"); } while (0)
#define BAR asm volatile("s_barrier" ::: "memory")
#define SCHED __builtin_amdgcn_sched_barrier(0)

DI bf16x8 tr_frag(LAS char* p0, LAS char* p1) {
  s16x4 a = __builtin_amdgcn_ds_read_tr16_b64_v4i16((LAS s16x4*)p0);
  s16x4 b = __builtin_amdgcn_ds_read_tr16_b64_v4i16((LAS s16x4*)p1);
  return __builtin_bit_cast(bf16x8, __builtin_shufflevector(a, b, 0, 1, 2, 3, 4, 5, 6, 7));
}
DI unsigned pk2(f32x2 v) { return __builtin_bit_cast(unsigned, __builtin_convertvector(v, bf16x2)); }
DI unsigned mish2(float h0, float h1) {
  const f32x2 h = {h0, h1};
  const f32x2 t = h * 1.44269504089f + (-14.f);
  const f32x2 E = {__builtin_amdgcn_fmed3f(__builtin_amdgcn_exp2f(t.x), 0.f, 1.f), __builtin_amdgcn_fmed3f(__builtin_amdgcn_exp2f(t.y), 0.f, 1.f)};
  const f32x2 W = E + 0x1p-13f;
  const f32x2 D = E * W + 0x1p-27f;
  const f32x2 R = {__builtin_amdgcn_rcpf(D.x), __builtin_amdgcn_rcpf(D.y)};
  return pk2(h * (E * (W * R)));
}
#define MFMA(a, b, c) __builtin_amdgcn_mfma_f32_16x16x32_bf16(a, b, c, 0, 0, 0)

DI constexpr int w2unit(int st) { constexpr int U[14] = {6, 7, 8, 9, 2, 5, 0, 3, 1, 4, 6, 7, 8, 9}; return U[st]; }

__global__ __launch_bounds__(768, 3) void mlp_fused(const char* __restrict__ x, const char* __restrict__ w1,
                                                    const unsigned* __restrict__ b1, const char* __restrict__ w2,
                                                    char* __restrict__ out, char* ws) {
  __shared__ __attribute__((aligned(1024))) char smem[163840];
  LAS char* sm = (LAS char*)smem;
  const unsigned lds0 = (unsigned)(size_t)sm;
  unsigned tlo = 0, thi = 0;
  STAMP(0);
  asm volatile("" :: "s"(x), "s"(w1), "s"(b1), "s"(w2), "s"(out), "s"(ws));
  const int b = blockIdx.x, xcd = b & 7, i = b >> 3;
  const int w = __builtin_amdgcn_readfirstlane(threadIdx.x >> 6), l = threadIdx.x & 63;
  const int c2 = i & 7;
  const int rt = (xcd >> 1) * 4 + (i & 3), ct = (xcd & 1) * 7 + (i >> 2);

  if (__builtin_expect(w >= 8, 1)) {
    __builtin_amdgcn_s_setprio(LPRIO);
    const int j = w - 8;
    const unsigned ldsw = lds0 + j * 4096;
    unsigned vw2[4];
    const char* w2c = w2 + c2 * 128;
    auto stage_w2 = [&](int st) { glds4(ldsw + w2unit(st) * UNIT, w2c + st * 131072, vw2); };
    auto thr = [&]() { if (THR >= 0) asm volatile("s_waitcnt vmcnt(%0)" :: "i"(THR >= 0 ? THR : 0) : "memory"); };
    if (__builtin_expect(i < 28, 1)) {
      unsigned vx[4], vw[4];
      vx[0] = (32 * j + (l >> 3)) * 1024 + (((l & 7) ^ (l >> 4)) * 16);
      vw[0] = (16 * j + (l >> 4)) * 3584 + (((l & 15) ^ ((l >> 4) << 2)) * 16);
#pragma unroll
      for (int k = 1; k < 4; k++) { vx[k] = (vx[0] ^ ((k & 1) * 64)) + k * 7168; vw[k] = (vw[0] ^ (k * 16)) + k * (4 * 3584 - 1024); }
      const char* xb = x + rt * 131072;
      const char* wb = w1 + ct * 256;
      auto stage1 = [&](int s) {
        glds4(ldsw + (s % 3) * UNIT, xb + s * 128, vx);
        glds4(ldsw + (3 + s % 3) * UNIT, wb + s * (64 * 3584), vw);
      };
      stage1(0);
      if (j == 0) asm volatile("s_mov_b32 m0, %2\n\ts_mov_b64 exec, 3\n\tglobal_load_lds_dword %0, %1 sc1\n\ts_mov_b64 exec, -1"
                               :: "v"(l * 4), "s"(ws + FLAG_OFF + 256 * 128), "s"(lds0 + 9 * UNIT) : "memory");
      if (j == 1) { u64 pc; asm volatile("s_getpc_b64 %0" : "=s"(pc));
        asm volatile("s_mov_b32 m0, %2\n\ts_mov_b64 exec, 0xff\n\tglobal_load_lds_dword %0, %1\n\ts_mov_b64 exec, -1"
                     :: "v"((unsigned)min(i * 7 + l, CODE_SECT - 1) * 64), "s"(pc), "s"(lds0 + 9 * UNIT + 4096) : "memory"); }
      BAR;
      stage1(1);
#pragma unroll
      for (int k = 0; k < 4; k++) vw2[k] = (32 * j + (l >> 3)) * 1024 + (((l & 7) ^ (((l >> 4) & 3) << 1)) * 16) + k * 7168;
#pragma unroll
      for (int s = 0; s < 8; s++) {
        VM_BAR(8);
        if (s < 6) stage1(s + 2);
        if (s == 6) { stage_w2(0); stage_w2(1); }
        if (s == 0) STAMP(2);
      }
#pragma unroll
      for (int st = 2; st < PRE; st++) stage_w2(st);
      STAMP(3);
      VM_BAR(4 * (PRE - 4));
      STAMP(5);
      BAR;
      STAMP(7);
#pragma unroll
      for (int st = PRE; st < 14; st++) { stage_w2(st); thr(); }
      STAMP(8);
    } else {
#pragma unroll
      for (int k = 0; k < 4; k++) vw2[k] = (32 * j + (l >> 3)) * 1024 + (((l & 7) ^ (((l >> 4) & 3) << 1)) * 16) + k * 7168;
#pragma nounroll
      for (int st = 0; st < 14; st++) {
        if (st == 4) VM_BAR(0);
        if (st == 10) BAR;
        const int u = (int)(0x98764130529876ull >> (4 * st)) & 15;
        if (HELP > 1 || (HELP == 1 && st < 10)) glds4(ldsw + u * UNIT, w2c - 512 + st * 131072, vw2);
        glds4(ldsw + u * UNIT, w2c + st * 131072, vw2);
        asm volatile("s_waitcnt vmcnt(16)" ::: "memory");
      }
    }
    BAR;
    VM_BAR(0);
    STAMP(12);
    if (b == 255 && j == 0) {
      GAS u64* flags = (GAS u64*)(ws + FLAG_OFF);
      const u64 tag = __hip_atomic_load(flags + 256 * 16, __ATOMIC_RELAXED, __HIP_MEMORY_SCOPE_AGENT) + 1;
#pragma nounroll
      for (int spins = 0; spins < (1 << 18); spins++) {
        u64 v[4];
#pragma unroll
        for (int k = 0; k < 4; k++) v[k] = __hip_atomic_load(flags + (k * 64 + l) * 16, __ATOMIC_RELAXED, __HIP_MEMORY_SCOPE_AGENT);
        if (__all(v[0] == tag && v[1] == tag && v[2] == tag && v[3] == tag)) break;
      }
      if (l == 0) __hip_atomic_store(flags + 256 * 16, tag, __ATOMIC_RELAXED, __HIP_MEMORY_SCOPE_AGENT);
    }
    return;
  }

  if (i < 28) BAR;
  STAMP(1);
  const int g = l >> 4, q4 = (l & 15) >> 2, p = l & 3;
  GAS u64* flags = (GAS u64*)(ws + FLAG_OFF);
  const __amdgpu_buffer_rsrc_t hrs = __builtin_amdgcn_make_buffer_rsrc(ws, 0, H_BYTES, 0x00020000);
  u64 tag;

  if (__builtin_expect(i < 28, 1)) {
    const int fq = w & 3, mh = w >> 2;
    f32x4 acc[2][4];
    { u32x16 bp;
      asm volatile("s_load_dwordx16 %0, %1, 0\n\ts_waitcnt lgkmcnt(0)" : "=&s"(bp) : "s"(b1 + (ct * 128 + fq * 32) / 2) : "memory");
#pragma unroll
      for (int fb = 0; fb < 2; fb++) {
        unsigned bb[2];
#pragma unroll
        for (int k = 0; k < 2; k++) {
          unsigned e0 = bp[8 * fb + k], e1 = bp[8 * fb + 2 + k], e2 = bp[8 * fb + 4 + k], e3 = bp[8 * fb + 6 + k];
          asm("" : "+s"(e0), "+s"(e1), "+s"(e2), "+s"(e3));
          bb[k] = (g & 2) ? ((g & 1) ? e3 : e2) : ((g & 1) ? e1 : e0);
        }
        const f32x4 bv = {__uint_as_float(bb[0] << 16), __uint_as_float(bb[0] & 0xffff0000u), __uint_as_float(bb[1] << 16), __uint_as_float(bb[1] & 0xffff0000u)};
#pragma unroll
        for (int m = 0; m < 4; m++) acc[fb][m] = bv;
      } }
    LAS char* xa[2];
#pragma unroll
    for (int ks = 0; ks < 2; ks++) xa[ks] = sm + (64 * mh + (l & 15)) * 128 + (((4 * ks + g) ^ ((l >> 1) & 7)) * 16);
    LAS char* wa[2][2];
#pragma unroll
    for (int fb = 0; fb < 2; fb++)
#pragma unroll
      for (int t = 0; t < 2; t++) {
        unsigned o = 3 * UNIT + (8 * g + 4 * t + q4) * 256 + (((4 * fq + 2 * fb + (p >> 1)) ^ ((q4 << 2) | ((2 * g + t) & 3))) * 16) + 8 * (p & 1);
        asm("" : "+v"(o));
        wa[fb][t] = sm + o;
      }
    bf16x8 a[2][2], bx[2][4];
    bf16x8 pa[2], pb[2];
    const unsigned hv = l * 16 + (((rt * 2 + mh) * 14 + ct) * 4 + fq) * 4096;
#pragma unroll
    for (int s = 0; s < 8; s++) {
      SCHED;
      LG_BAR;
      if (s == 0) STAMP(2);
      if (s == 7) STAMP(3);
      if (s == 1) tag = *(volatile LAS u64*)(sm + 9 * UNIT) + 1;
      const unsigned so = (s % 3) * UNIT;
      auto rda = [&](int ks, int fb) { a[ks][fb] = tr_frag(wa[fb][0] + so + ks * 8192, wa[fb][1] + so + ks * 8192); SCHED; };
      auto rdb = [&](int ks, int m) { bx[ks][m] = *(LAS bf16x8*)(xa[ks] + so + m * 2048); bx[ks][m + 1] = *(LAS bf16x8*)(xa[ks] + so + m * 2048 + 2048); SCHED; };
      auto mm = [&](int ks, int fb, int m) { acc[fb][m] = MFMA(a[ks][fb], bx[ks][m], acc[fb][m]); SCHED; };
      auto late = [&](int fb, int m) { if (s > 0) { acc[fb][2 + m] = MFMA(pa[fb], pb[m], acc[fb][2 + m]); SCHED; } };
      late(0, 0); rda(0, 0); late(1, 0); rdb(0, 0); late(0, 1); rda(0, 1); late(1, 1); rdb(0, 2);
      if (s < 7) {
        mm(0, 0, 0); rda(1, 0); mm(0, 0, 1); rdb(1, 0); mm(0, 1, 0); rda(1, 1); mm(0, 1, 1); rdb(1, 2);
        mm(0, 0, 2); mm(0, 0, 3); mm(0, 1, 2); mm(0, 1, 3);
        mm(1, 0, 0); mm(1, 0, 1); mm(1, 1, 0); mm(1, 1, 1);
        pa[0] = a[1][0]; pa[1] = a[1][1]; pb[0] = bx[1][2]; pb[1] = bx[1][3];
      } else {
        mm(0, 0, 0); rda(1, 0); mm(0, 1, 0); rdb(1, 0); mm(0, 0, 1); rda(1, 1); mm(0, 1, 1); rdb(1, 2);
        mm(1, 0, 0); mm(1, 1, 0); mm(1, 0, 1); mm(1, 1, 1);
      }
    }
    SCHED;
    STAMP(4);
#pragma unroll
    for (int m = 0; m < 4; m++) {
      if (m < 2) {
#pragma unroll
        for (int ks = 0; ks < 2; ks++)
#pragma unroll
          for (int fb = 0; fb < 2; fb++) acc[fb][2 + m] = MFMA(a[ks][fb], bx[ks][2 + m], acc[fb][2 + m]);
      }
      const u32x4 o = {mish2(acc[0][m][0], acc[0][m][1]), mish2(acc[0][m][2], acc[0][m][3]), mish2(acc[1][m][0], acc[1][m][1]), mish2(acc[1][m][2], acc[1][m][3])};
      __builtin_amdgcn_raw_buffer_store_b128(o, hrs, hv + m * 1024, 0, 16);
      SCHED;
    }
    STAMP(5);
  } else {
    tag = __hip_atomic_load(flags + 256 * 16, __ATOMIC_RELAXED, __HIP_MEMORY_SCOPE_AGENT) + 1;
    const int hid = xcd * 4 + (i - 28);
    if (w == 0 && l == 0) __hip_atomic_store(flags + ((hid >> 1) * 16 + 14 + (hid & 1)) * 16, tag, __ATOMIC_RELAXED, __HIP_MEMORY_SCOPE_AGENT);
  }
  SCHED;
  BAR;
  const int r2 = xcd * 4 + (i >> 3);
  {
    const int q = w & 3, mb = w >> 2;
    LAS char* wa[3][4];
#pragma unroll
    for (int k = 0; k < 3; k++)
#pragma unroll
      for (int c = 0; c < 4; c++) {
        unsigned o = k * 65536 + (32 * q + 4 * g + q4) * 128 + (((2 * c + (p >> 1)) ^ ((((2 * g) | (q4 >> 1)) & 3) << 1)) * 16) + 8 * (p & 1);
        asm("" : "+v"(o));
        wa[k][c] = sm + o;
      }
    bf16x8 af[14][4];
    auto read_w = [&](int st, int c) {
      const int u = w2unit(st);
      LAS char* a = wa[u >> 2][c] + (u & 3) * UNIT;
      af[st][c] = tr_frag(a, a + 2048);
    };
#pragma unroll
    for (int st = 0; st < 4; st++)
#pragma unroll
      for (int c = 0; c < 4; c++) read_w(st, c);
    SCHED;
    asm volatile("s_waitcnt vmcnt(0)" ::: "memory");
    STAMP(6);
    LG_BAR;
    if (w == 0 && i < 28 && l == 0) __hip_atomic_store(flags + (rt * 16 + ct) * 16, tag, __ATOMIC_RELAXED, __HIP_MEMORY_SCOPE_AGENT);
    STAMP(15);
    if (w == POLLW) {
      GAS u64* fp = flags + ((r2 >> 1) * 16 + (l < 14 ? l : 0)) * 16;
#pragma nounroll
      for (int spins = 0; spins < (1 << 18); spins++) {
        if (__all(__hip_atomic_load(fp, __ATOMIC_RELAXED, __HIP_MEMORY_SCOPE_AGENT) == tag)) break;
      }
      STAMP(14);
    }
    if (w == 2) {
      GAS u64* fp = flags + ((r2 >> 1) * 16 + (l < 14 ? l : 0)) * 16;
      unsigned done = 0;
      u32x4 d0 = {}, d1 = {};
#pragma nounroll
      for (int spins = 0; spins < (1 << 18); spins++) {
        const unsigned rdy = (unsigned)__ballot(__hip_atomic_load(fp, __ATOMIC_RELAXED, __HIP_MEMORY_SCOPE_AGENT) == tag) & 0x3fff;
        asm volatile("" :: "v"(d0), "v"(d1));
        if (rdy == 0x3fff) break;
        unsigned nw = rdy & ~done; done = rdy;
#pragma nounroll
        while (nw) {
          const int k = __builtin_ctz(nw); nw &= nw - 1;
          const unsigned so = (r2 * 14 + k) * UNIT + c2 * 2048;
          d0 = __builtin_amdgcn_raw_buffer_load_b128(hrs, l * 16, so, 16);
          d1 = __builtin_amdgcn_raw_buffer_load_b128(hrs, l * 16 + 1024, so, 16);
        }
      }
    }
    SCHED;
    BAR;
    STAMP(7);
    const unsigned hv = l * 16 + (q * 2 + mb) * 2048;
    const unsigned hs = r2 * 14 * UNIT;
    u32x4 hf[14][2];
    auto load_h = [&](int st) {
      hf[st][0] = __builtin_amdgcn_raw_buffer_load_b128(hrs, hv, hs + st * UNIT, 16);
      hf[st][1] = __builtin_amdgcn_raw_buffer_load_b128(hrs, hv + 1024, hs + st * UNIT, 16);
    };
#pragma unroll
    for (int st = 0; st < DEP; st++) load_h(st);
    SCHED;
    STAMP(8);
    f32x4 acc[4][2] = {};
#pragma unroll
    for (int st = 0; st < 14; st++) {
      SCHED;
      if (st == 3) { BAR; STAMP(13); }
      if (st == 9) STAMP(9);
#pragma unroll
      for (int m = 0; m < 2; m++)
#pragma unroll
        for (int c = 0; c < 4; c++) {
          acc[c][m] = MFMA(af[st][c], __builtin_bit_cast(bf16x8, hf[st][m]), acc[c][m]);
          SCHED;
          if (st >= 3 && st + 1 < 14 && m == 0) { read_w(st + 1, c); SCHED; }
        }
      if (st + DEP < 14) load_h(st + DEP);
    }
    SCHED;
    STAMP(10);
    BAR;
    LAS char* rw = sm + q * RED_Q + (32 * mb + (l & 15)) * 272 + g * 16;
#pragma unroll
    for (int m = 0; m < 2; m++)
#pragma unroll
      for (int c = 0; c < 4; c++) *(LAS f32x4*)(rw + m * (16 * 272) + c * 64) = acc[c][m];
    LG_BAR;
    LAS char* rr = sm + (8 * w + (l >> 3)) * 272 + (l & 7) * 32;
    f32x4 s0 = *(LAS f32x4*)rr, s1 = *(LAS f32x4*)(rr + 16);
#pragma unroll
    for (int k = 1; k < 4; k++) { s0 += *(LAS f32x4*)(rr + k * RED_Q); s1 += *(LAS f32x4*)(rr + k * RED_Q + 16); }
    const u32x4 o = {pk2((f32x2){s0[0], s0[1]}), pk2((f32x2){s0[2], s0[3]}), pk2((f32x2){s1[0], s1[1]}), pk2((f32x2){s1[2], s1[3]})};
    *(u32x4*)(out + (size_t)(r2 * 64 + 8 * w + (l >> 3)) * 1024 + c2 * 128 + (l & 7) * 16) = o;
  }
  STAMP(11);
  asm volatile("s_waitcnt vmcnt(0)" ::: "memory");
  STAMP(12);
  if (w == AMPW) {
    GAS u64* t0s = (GAS u64*)(ws + (16u << 20));
    if (l == 0) __hip_atomic_store(t0s + b, TS(0), __ATOMIC_RELAXED, __HIP_MEMORY_SCOPE_AGENT);
    u64 t1; u64 tgt = TS(0) + 2000;
    do { asm volatile("s_memrealtime %0\n\ts_waitcnt lgkmcnt(0)" : "=s"(t1) :: "memory"); } while (t1 < tgt);
    if (b == 0) {
      const char* pb = ws + (100u << 20);
      u64 ta, tb, tc; unsigned d;
      asm volatile("s_memrealtime %0\n\ts_waitcnt lgkmcnt(0)" : "=s"(ta) :: "memory");
      asm volatile("global_load_dword %0, %1, %2\n\ts_waitcnt vmcnt(0)" : "=v"(d) : "v"(0), "s"(pb) : "memory");
      asm volatile("s_memrealtime %0\n\ts_waitcnt lgkmcnt(0)" : "=s"(tb) :: "memory");
      asm volatile("global_load_dword %0, %1, %2\n\ts_waitcnt vmcnt(0)" : "=v"(d) : "v"(PROBE), "s"(pb) : "memory");
      asm volatile("s_memrealtime %0\n\ts_waitcnt lgkmcnt(0)" : "=s"(tc) :: "memory");
      tgt = TS(0) + 3000 + 32 * (PROBE ? tc - tb : tb - ta);
      do { asm volatile("s_memrealtime %0\n\ts_waitcnt lgkmcnt(0)" : "=s"(t1) :: "memory"); } while (t1 < tgt);
    }
    if (AMPSEL) {
      unsigned mn = 0xffffffffu;
      for (int k = 0; k < 4; k++) mn = min(mn, (unsigned)(__hip_atomic_load(t0s + k * 64 + l, __ATOMIC_RELAXED, __HIP_MEMORY_SCOPE_AGENT) - (TS(0) - 100000)));
      for (int o = 32; o; o >>= 1) mn = min(mn, (unsigned)__shfl_xor((int)mn, o));
      const u64 base = TS(0) - 100000 + __builtin_amdgcn_readfirstlane(mn);
      tgt = base + 3000 + 8 * (TS(AMP) - base);
      if (tgt > base + 30000) tgt = base + 30000;
      do { asm volatile("s_memrealtime %0\n\ts_waitcnt lgkmcnt(0)" : "=s"(t1) :: "memory"); } while (t1 < tgt);
    }
  }
}

extern "C" void kernel_launch(void* const* d_in, const int* in_sizes, int n_in, void* d_out, int out_size,
                              void* d_ws, size_t ws_size, hipStream_t stream) {
  mlp_fused<<<256, 768, 0, stream>>>((const char*)d_in[0], (const char*)d_in[1], (const unsigned*)d_in[2],
                                     (const char*)d_in[3], (char*)d_out, (char*)d_ws + WSOFF);
}
```

```cpp
#define STAMPS
#define AMP 0
#define AMPSEL false
#define PROBE0 w1
#include <hip/hip_runtime.h>
#include <stdint.h>

typedef short    s16x4  __attribute__((ext_vector_type(4)));
typedef __bf16   bf16x8 __attribute__((ext_vector_type(8)));
typedef __bf16   bf16x2 __attribute__((ext_vector_type(2)));
typedef float    f32x2  __attribute__((ext_vector_type(2)));
typedef float    f32x4  __attribute__((ext_vector_type(4)));
typedef unsigned u32x4  __attribute__((ext_vector_type(4)));
typedef unsigned u32x2  __attribute__((ext_vector_type(2)));
typedef unsigned u32x16 __attribute__((ext_vector_type(16)));
typedef unsigned long long u64;
#define LAS __attribute__((address_space(3)))
#define GAS __attribute__((address_space(1)))
#define DI __device__ __forceinline__

#define AMPW 0
#define WSOFF 0
#define HELP 2
#define CODEPF 1
#define DEP 4
#define PSLEEP 0
#define THR -1
#define LPRIO 0
#define POLLW 1
#define HPF 1
#define PRE 8

constexpr unsigned UNIT = 16384;
constexpr unsigned H_BYTES = 32u * 14u * 16384u;
constexpr unsigned FLAG_OFF = 8u << 20;
constexpr int CODE_SECT = 192;
constexpr unsigned RED_Q = 64u * 272u;

#define STAMP_INS "s_memrealtime"
#define STAMP(i) do { u64 t_; asm volatile(STAMP_INS " %0\n\ts_waitcnt lgkmcnt(0)" : "=s"(t_) :: "memory"); \
    asm volatile("v_writelane_b32 %0, %2, %4\n\tv_writelane_b32 %1, %3, %4" : "+v"(tlo), "+v"(thi) : "s"((unsigned)t_), "s"((unsigned)(t_ >> 32)), "i"(i)); } while (0)
#define STAMP_OUT do { if (l < 16) ((u64*)(ws + (16u << 20)))[(b * 16 + w) * 16 + l] = ((u64)thi << 32) | tlo; } while (0)
#define TS(i) (((u64)(unsigned)__builtin_amdgcn_readlane(thi, i) << 32) | (unsigned)__builtin_amdgcn_readlane(tlo, i))

DI void glds4(unsigned ldsaddr, const char* base, const unsigned (&v)[4]) {
  asm volatile("s_mov_b32 m0, %0\n\ts_nop 0\n\t"
               "global_load_lds_dwordx4 %2, %1\n\t"
               "global_load_lds_dwordx4 %3, %1 offset:1024\n\t"
               "global_load_lds_dwordx4 %4, %1 offset:2048\n\t"
               "global_load_lds_dwordx4 %5, %1 offset:3072"
               :: "s"(__builtin_amdgcn_readfirstlane(ldsaddr)), "s"(base), "v"(v[0]), "v"(v[1]), "v"(v[2]), "v"(v[3]) : "memory");
}
#define VM_BAR(n) asm volatile("s_waitcnt vmcnt(%0)\n\ts_barrier" :: "i"(n) : "memory")
#define LG_BAR do { __builtin_amdgcn_s_waitcnt(0xC07F); asm volatile("s_barrier" ::: "memory"); } while (0)
#define BAR asm volatile("s_barrier" ::: "memory")
#define SCHED __builtin_amdgcn_sched_barrier(0)

DI bf16x8 tr_frag(LAS char* p0, LAS char* p1) {
  s16x4 a = __builtin_amdgcn_ds_read_tr16_b64_v4i16((LAS s16x4*)p0);
  s16x4 b = __builtin_amdgcn_ds_read_tr16_b64_v4i16((LAS s16x4*)p1);
  return __builtin_bit_cast(bf16x8, __builtin_shufflevector(a, b, 0, 1, 2, 3, 4, 5, 6, 7));
}
DI unsigned pk2(f32x2 v) { return __builtin_bit_cast(unsigned, __builtin_convertvector(v, bf16x2)); }
DI unsigned mish2(float h0, float h1) {
  const f32x2 h = {h0, h1};
  const f32x2 t = h * 1.44269504089f + (-14.f);
  const f32x2 E = {__builtin_amdgcn_fmed3f(__builtin_amdgcn_exp2f(t.x), 0.f, 1.f), __builtin_amdgcn_fmed3f(__builtin_amdgcn_exp2f(t.y), 0.f, 1.f)};
  const f32x2 W = E + 0x1p-13f;
  const f32x2 D = E * W + 0x1p-27f;
  const f32x2 R = {__builtin_amdgcn_rcpf(D.x), __builtin_amdgcn_rcpf(D.y)};
  return pk2(h * (E * (W * R)));
}
#define MFMA(a, b, c) __builtin_amdgcn_mfma_f32_16x16x32_bf16(a, b, c, 0, 0, 0)

DI constexpr int w2unit(int st) { constexpr int U[14] = {6, 7, 8, 9, 2, 5, 0, 3, 1, 4, 6, 7, 8, 9}; return U[st]; }

__global__ __launch_bounds__(768, 3) void mlp_fused(const char* __restrict__ x, const char* __restrict__ w1,
                                                    const unsigned* __restrict__ b1, const char* __restrict__ w2,
                                                    char* __restrict__ out, char* ws) {
  __shared__ __attribute__((aligned(1024))) char smem[163840];
  LAS char* sm = (LAS char*)smem;
  const unsigned lds0 = (unsigned)(size_t)sm;
  unsigned tlo = 0, thi = 0;
  STAMP(0);
  asm volatile("" :: "s"(x), "s"(w1), "s"(b1), "s"(w2), "s"(out), "s"(ws));
  const int b = blockIdx.x, xcd = b & 7, i = b >> 3;
  const int w = __builtin_amdgcn_readfirstlane(threadIdx.x >> 6), l = threadIdx.x & 63;
  u64 plat = 0;
  { u64 ta, tb;
    if (b == 0 && w == 0) {
      const char* pb = PROBE0;
      unsigned d = 0;
      asm volatile("s_memrealtime %0\n\ts_waitcnt lgkmcnt(0)" : "=s"(ta) :: "memory");
#pragma nounroll
      for (int k = 0; k < 16; k++) asm volatile("global_load_dword %0, %1, %2\n\ts_waitcnt vmcnt(0)" : "=v"(d) : "v"(k * 61632 + 4096), "s"(pb) : "memory");
      asm volatile("s_memrealtime %0\n\ts_waitcnt lgkmcnt(0)" : "=s"(tb) :: "memory");
      plat = tb - ta;
    }
    const u64 tg = TS(0) + 1500;
    do { asm volatile("s_memrealtime %0\n\ts_waitcnt lgkmcnt(0)" : "=s"(tb) :: "memory"); } while (tb < tg);
  }
  const int c2 = i & 7;
  const int rt = (xcd >> 1) * 4 + (i & 3), ct = (xcd & 1) * 7 + (i >> 2);

  if (__builtin_expect(w >= 8, 1)) {
    __builtin_amdgcn_s_setprio(LPRIO);
    const int j = w - 8;
    const unsigned ldsw = lds0 + j * 4096;
    unsigned vw2[4];
    const char* w2c = w2 + c2 * 128;
    auto stage_w2 = [&](int st) { glds4(ldsw + w2unit(st) * UNIT, w2c + st * 131072, vw2); };
    auto thr = [&]() { if (THR >= 0) asm volatile("s_waitcnt vmcnt(%0)" :: "i"(THR >= 0 ? THR : 0) : "memory"); };
    if (__builtin_expect(i < 28, 1)) {
      unsigned vx[4], vw[4];
      vx[0] = (32 * j + (l >> 3)) * 1024 + (((l & 7) ^ (l >> 4)) * 16);
      vw[0] = (16 * j + (l >> 4)) * 3584 + (((l & 15) ^ ((l >> 4) << 2)) * 16);
#pragma unroll
      for (int k = 1; k < 4; k++) { vx[k] = (vx[0] ^ ((k & 1) * 64)) + k * 7168; vw[k] = (vw[0] ^ (k * 16)) + k * (4 * 3584 - 1024); }
      const char* xb = x + rt * 131072;
      const char* wb = w1 + ct * 256;
      auto stage1 = [&](int s) {
        glds4(ldsw + (s % 3) * UNIT, xb + s * 128, vx);
        glds4(ldsw + (3 + s % 3) * UNIT, wb + s * (64 * 3584), vw);
      };
      stage1(0);
      if (j == 0) asm volatile("s_mov_b32 m0, %2\n\ts_mov_b64 exec, 3\n\tglobal_load_lds_dword %0, %1 sc1\n\ts_mov_b64 exec, -1"
                               :: "v"(l * 4), "s"(ws + FLAG_OFF + 256 * 128), "s"(lds0 + 9 * UNIT) : "memory");
      if (j == 1) { u64 pc; asm volatile("s_getpc_b64 %0" : "=s"(pc));
        asm volatile("s_mov_b32 m0, %2\n\ts_mov_b64 exec, 0xff\n\tglobal_load_lds_dword %0, %1\n\ts_mov_b64 exec, -1"
                     :: "v"((unsigned)min(i * 7 + l, CODE_SECT - 1) * 64), "s"(pc), "s"(lds0 + 9 * UNIT + 4096) : "memory"); }
      BAR;
      stage1(1);
#pragma unroll
      for (int k = 0; k < 4; k++) vw2[k] = (32 * j + (l >> 3)) * 1024 + (((l & 7) ^ (((l >> 4) & 3) << 1)) * 16) + k * 7168;
#pragma unroll
      for (int s = 0; s < 8; s++) {
        VM_BAR(8);
        if (s < 6) stage1(s + 2);
        if (s == 6) { stage_w2(0); stage_w2(1); }
        if (s == 0) STAMP(2);
      }
#pragma unroll
      for (int st = 2; st < PRE; st++) stage_w2(st);
      STAMP(3);
      VM_BAR(4 * (PRE - 4));
      STAMP(5);
      BAR;
      STAMP(7);
#pragma unroll
      for (int st = PRE; st < 14; st++) { stage_w2(st); thr(); }
      STAMP(8);
    } else {
#pragma unroll
      for (int k = 0; k < 4; k++) vw2[k] = (32 * j + (l >> 3)) * 1024 + (((l & 7) ^ (((l >> 4) & 3) << 1)) * 16) + k * 7168;
#pragma nounroll
      for (int st = 0; st < 14; st++) {
        if (st == 4) VM_BAR(0);
        if (st == 10) BAR;
        const int u = (int)(0x98764130529876ull >> (4 * st)) & 15;
        if (HELP > 1 || (HELP == 1 && st < 10)) glds4(ldsw + u * UNIT, w2c - 512 + st * 131072, vw2);
        glds4(ldsw + u * UNIT, w2c + st * 131072, vw2);
        asm volatile("s_waitcnt vmcnt(16)" ::: "memory");
      }
    }
    BAR;
    VM_BAR(0);
    STAMP(12);
    if (b == 255 && j == 0) {
      GAS u64* flags = (GAS u64*)(ws + FLAG_OFF);
      const u64 tag = __hip_atomic_load(flags + 256 * 16, __ATOMIC_RELAXED, __HIP_MEMORY_SCOPE_AGENT) + 1;
#pragma nounroll
      for (int spins = 0; spins < (1 << 18); spins++) {
        u64 v[4];
#pragma unroll
        for (int k = 0; k < 4; k++) v[k] = __hip_atomic_load(flags + (k * 64 + l) * 16, __ATOMIC_RELAXED, __HIP_MEMORY_SCOPE_AGENT);
        if (__all(v[0] == tag && v[1] == tag && v[2] == tag && v[3] == tag)) break;
      }
      if (l == 0) __hip_atomic_store(flags + 256 * 16, tag, __ATOMIC_RELAXED, __HIP_MEMORY_SCOPE_AGENT);
    }
    return;
  }

  if (i < 28) BAR;
  STAMP(1);
  const int g = l >> 4, q4 = (l & 15) >> 2, p = l & 3;
  GAS u64* flags = (GAS u64*)(ws + FLAG_OFF);
  const __amdgpu_buffer_rsrc_t hrs = __builtin_amdgcn_make_buffer_rsrc(ws, 0, H_BYTES, 0x00020000);
  u64 tag;

  if (__builtin_expect(i < 28, 1)) {
    const int fq = w & 3, mh = w >> 2;
    f32x4 acc[2][4];
    { u32x16 bp;
      asm volatile("s_load_dwordx16 %0, %1, 0\n\ts_waitcnt lgkmcnt(0)" : "=&s"(bp) : "s"(b1 + (ct * 128 + fq * 32) / 2) : "memory");
#pragma unroll
      for (int fb = 0; fb < 2; fb++) {
        unsigned bb[2];
#pragma unroll
        for (int k = 0; k < 2; k++) {
          unsigned e0 = bp[8 * fb + k], e1 = bp[8 * fb + 2 + k], e2 = bp[8 * fb + 4 + k], e3 = bp[8 * fb + 6 + k];
          asm("" : "+s"(e0), "+s"(e1), "+s"(e2), "+s"(e3));
          bb[k] = (g & 2) ? ((g & 1) ? e3 : e2) : ((g & 1) ? e1 : e0);
        }
        const f32x4 bv = {__uint_as_float(bb[0] << 16), __uint_as_float(bb[0] & 0xffff0000u), __uint_as_float(bb[1] << 16), __uint_as_float(bb[1] & 0xffff0000u)};
#pragma unroll
        for (int m = 0; m < 4; m++) acc[fb][m] = bv;
      } }
    LAS char* xa[2];
#pragma unroll
    for (int ks = 0; ks < 2; ks++) xa[ks] = sm + (64 * mh + (l & 15)) * 128 + (((4 * ks + g) ^ ((l >> 1) & 7)) * 16);
    LAS char* wa[2][2];
#pragma unroll
    for (int fb = 0; fb < 2; fb++)
#pragma unroll
      for (int t = 0; t < 2; t++) {
        unsigned o = 3 * UNIT + (8 * g + 4 * t + q4) * 256 + (((4 * fq + 2 * fb + (p >> 1)) ^ ((q4 << 2) | ((2 * g + t) & 3))) * 16) + 8 * (p & 1);
        asm("" : "+v"(o));
        wa[fb][t] = sm + o;
      }
    bf16x8 a[2][2], bx[2][4];
    bf16x8 pa[2], pb[2];
    const unsigned hv = l * 16 + (((rt * 2 + mh) * 14 + ct) * 4 + fq) * 4096;
#pragma unroll
    for (int s = 0; s < 8; s++) {
      SCHED;
      LG_BAR;
      if (s == 0) STAMP(2);
      if (s == 7) STAMP(3);
      if (s == 1) tag = *(volatile LAS u64*)(sm + 9 * UNIT) + 1;
      const unsigned so = (s % 3) * UNIT;
      auto rda = [&](int ks, int fb) { a[ks][fb] = tr_frag(wa[fb][0] + so + ks * 8192, wa[fb][1] + so + ks * 8192); SCHED; };
      auto rdb = [&](int ks, int m) { bx[ks][m] = *(LAS bf16x8*)(xa[ks] + so + m * 2048); bx[ks][m + 1] = *(LAS bf16x8*)(xa[ks] + so + m * 2048 + 2048); SCHED; };
      auto mm = [&](int ks, int fb, int m) { acc[fb][m] = MFMA(a[ks][fb], bx[ks][m], acc[fb][m]); SCHED; };
      auto late = [&](int fb, int m) { if (s > 0) { acc[fb][2 + m] = MFMA(pa[fb], pb[m], acc[fb][2 + m]); SCHED; } };
      late(0, 0); rda(0, 0); late(1, 0); rdb(0, 0); late(0, 1); rda(0, 1); late(1, 1); rdb(0, 2);
      if (s < 7) {
        mm(0, 0, 0); rda(1, 0); mm(0, 0, 1); rdb(1, 0); mm(0, 1, 0); rda(1, 1); mm(0, 1, 1); rdb(1, 2);
        mm(0, 0, 2); mm(0, 0, 3); mm(0, 1, 2); mm(0, 1, 3);
        mm(1, 0, 0); mm(1, 0, 1); mm(1, 1, 0); mm(1, 1, 1);
        pa[0] = a[1][0]; pa[1] = a[1][1]; pb[0] = bx[1][2]; pb[1] = bx[1][3];
      } else {
        mm(0, 0, 0); rda(1, 0); mm(0, 1, 0); rdb(1, 0); mm(0, 0, 1); rda(1, 1); mm(0, 1, 1); rdb(1, 2);
        mm(1, 0, 0); mm(1, 1, 0); mm(1, 0, 1); mm(1, 1, 1);
      }
    }
    SCHED;
    STAMP(4);
#pragma unroll
    for (int m = 0; m < 4; m++) {
      if (m < 2) {
#pragma unroll
        for (int ks = 0; ks < 2; ks++)
#pragma unroll
          for (int fb = 0; fb < 2; fb++) acc[fb][2 + m] = MFMA(a[ks][fb], bx[ks][2 + m], acc[fb][2 + m]);
      }
      const u32x4 o = {mish2(acc[0][m][0], acc[0][m][1]), mish2(acc[0][m][2], acc[0][m][3]), mish2(acc[1][m][0], acc[1][m][1]), mish2(acc[1][m][2], acc[1][m][3])};
      __builtin_amdgcn_raw_buffer_store_b128(o, hrs, hv + m * 1024, 0, 16);
      SCHED;
    }
    STAMP(5);
  } else {
    tag = __hip_atomic_load(flags + 256 * 16, __ATOMIC_RELAXED, __HIP_MEMORY_SCOPE_AGENT) + 1;
    const int hid = xcd * 4 + (i - 28);
    if (w == 0 && l == 0) __hip_atomic_store(flags + ((hid >> 1) * 16 + 14 + (hid & 1)) * 16, tag, __ATOMIC_RELAXED, __HIP_MEMORY_SCOPE_AGENT);
  }
  SCHED;
  BAR;
  const int r2 = xcd * 4 + (i >> 3);
  {
    const int q = w & 3, mb = w >> 2;
    LAS char* wa[3][4];
#pragma unroll
    for (int k = 0; k < 3; k++)
#pragma unroll
      for (int c = 0; c < 4; c++) {
        unsigned o = k * 65536 + (32 * q + 4 * g + q4) * 128 + (((2 * c + (p >> 1)) ^ ((((2 * g) | (q4 >> 1)) & 3) << 1)) * 16) + 8 * (p & 1);
        asm("" : "+v"(o));
        wa[k][c] = sm + o;
      }
    bf16x8 af[14][4];
    auto read_w = [&](int st, int c) {
      const int u = w2unit(st);
      LAS char* a = wa[u >> 2][c] + (u & 3) * UNIT;
      af[st][c] = tr_frag(a, a + 2048);
    };
#pragma unroll
    for (int st = 0; st < 4; st++)
#pragma unroll
      for (int c = 0; c < 4; c++) read_w(st, c);
    SCHED;
    asm volatile("s_waitcnt vmcnt(0)" ::: "memory");
    STAMP(6);
    LG_BAR;
    if (w == 0 && i < 28 && l == 0) __hip_atomic_store(flags + (rt * 16 + ct) * 16, tag, __ATOMIC_RELAXED, __HIP_MEMORY_SCOPE_AGENT);
    STAMP(15);
    if (w == POLLW) {
      GAS u64* fp = flags + ((r2 >> 1) * 16 + (l < 14 ? l : 0)) * 16;
#pragma nounroll
      for (int spins = 0; spins < (1 << 18); spins++) {
        if (__all(__hip_atomic_load(fp, __ATOMIC_RELAXED, __HIP_MEMORY_SCOPE_AGENT) == tag)) break;
      }
      STAMP(14);
    }
    if (w == 2) {
      GAS u64* fp = flags + ((r2 >> 1) * 16 + (l < 14 ? l : 0)) * 16;
      unsigned done = 0;
      u32x4 d0 = {}, d1 = {};
#pragma nounroll
      for (int spins = 0; spins < (1 << 18); spins++) {
        const unsigned rdy = (unsigned)__ballot(__hip_atomic_load(fp, __ATOMIC_RELAXED, __HIP_MEMORY_SCOPE_AGENT) == tag) & 0x3fff;
        asm volatile("" :: "v"(d0), "v"(d1));
        if (rdy == 0x3fff) break;
        unsigned nw = rdy & ~done; done = rdy;
#pragma nounroll
        while (nw) {
          const int k = __builtin_ctz(nw); nw &= nw - 1;
          const unsigned so = (r2 * 14 + k) * UNIT + c2 * 2048;
          d0 = __builtin_amdgcn_raw_buffer_load_b128(hrs, l * 16, so, 16);
          d1 = __builtin_amdgcn_raw_buffer_load_b128(hrs, l * 16 + 1024, so, 16);
        }
      }
    }
    SCHED;
    BAR;
    STAMP(7);
    const unsigned hv = l * 16 + (q * 2 + mb) * 2048;
    const unsigned hs = r2 * 14 * UNIT;
    u32x4 hf[14][2];
    auto load_h = [&](int st) {
      hf[st][0] = __builtin_amdgcn_raw_buffer_load_b128(hrs, hv, hs + st * UNIT, 16);
      hf[st][1] = __builtin_amdgcn_raw_buffer_load_b128(hrs, hv + 1024, hs + st * UNIT, 16);
    };
#pragma unroll
    for (int st = 0; st < DEP; st++) load_h(st);
    SCHED;
    STAMP(8);
    f32x4 acc[4][2] = {};
#pragma unroll
    for (int st = 0; st < 14; st++) {
      SCHED;
      if (st == 3) { BAR; STAMP(13); }
      if (st == 9) STAMP(9);
#pragma unroll
      for (int m = 0; m < 2; m++)
#pragma unroll
        for (int c = 0; c < 4; c++) {
          acc[c][m] = MFMA(af[st][c], __builtin_bit_cast(bf16x8, hf[st][m]), acc[c][m]);
          SCHED;
          if (st >= 3 && st + 1 < 14 && m == 0) { read_w(st + 1, c); SCHED; }
        }
      if (st + DEP < 14) load_h(st + DEP);
    }
    SCHED;
    STAMP(10);
    BAR;
    LAS char* rw = sm + q * RED_Q + (32 * mb + (l & 15)) * 272 + g * 16;
#pragma unroll
    for (int m = 0; m < 2; m++)
#pragma unroll
      for (int c = 0; c < 4; c++) *(LAS f32x4*)(rw + m * (16 * 272) + c * 64) = acc[c][m];
    LG_BAR;
    LAS char* rr = sm + (8 * w + (l >> 3)) * 272 + (l & 7) * 32;
    f32x4 s0 = *(LAS f32x4*)rr, s1 = *(LAS f32x4*)(rr + 16);
#pragma unroll
    for (int k = 1; k < 4; k++) { s0 += *(LAS f32x4*)(rr + k * RED_Q); s1 += *(LAS f32x4*)(rr + k * RED_Q + 16); }
    const u32x4 o = {pk2((f32x2){s0[0], s0[1]}), pk2((f32x2){s0[2], s0[3]}), pk2((f32x2){s1[0], s1[1]}), pk2((f32x2){s1[2], s1[3]})};
    *(u32x4*)(out + (size_t)(r2 * 64 + 8 * w + (l >> 3)) * 1024 + c2 * 128 + (l & 7) * 16) = o;
  }
  STAMP(11);
  asm volatile("s_waitcnt vmcnt(0)" ::: "memory");
  STAMP(12);
  if (w == AMPW) {
    GAS u64* t0s = (GAS u64*)(ws + (16u << 20));
    if (l == 0) __hip_atomic_store(t0s + b, TS(0), __ATOMIC_RELAXED, __HIP_MEMORY_SCOPE_AGENT);
    u64 t1; u64 tgt = TS(0) + 2000;
    do { asm volatile("s_memrealtime %0\n\ts_waitcnt lgkmcnt(0)" : "=s"(t1) :: "memory"); } while (t1 < tgt);
    if (b == 0) { tgt = TS(0) + 4000 + 4 * plat; do { asm volatile("s_memrealtime %0\n\ts_waitcnt lgkmcnt(0)" : "=s"(t1) :: "memory"); } while (t1 < tgt); }
    if (AMPSEL) {
      unsigned mn = 0xffffffffu;
      for (int k = 0; k < 4; k++) mn = min(mn, (unsigned)(__hip_atomic_load(t0s + k * 64 + l, __ATOMIC_RELAXED, __HIP_MEMORY_SCOPE_AGENT) - (TS(0) - 100000)));
      for (int o = 32; o; o >>= 1) mn = min(mn, (unsigned)__shfl_xor((int)mn, o));
      const u64 base = TS(0) - 100000 + __builtin_amdgcn_readfirstlane(mn);
      tgt = base + 3000 + 8 * (TS(AMP) - base);
      if (tgt > base + 30000) tgt = base + 30000;
      do { asm volatile("s_memrealtime %0\n\ts_waitcnt lgkmcnt(0)" : "=s"(t1) :: "memory"); } while (t1 < tgt);
    }
  }
}

extern "C" void kernel_launch(void* const* d_in, const int* in_sizes, int n_in, void* d_out, int out_size,
                              void* d_ws, size_t ws_size, hipStream_t stream) {
  mlp_fused<<<256, 768, 0, stream>>>((const char*)d_in[0], (const char*)d_in[1], (const unsigned*)d_in[2],
                                     (const char*)d_in[3], (char*)d_out, (char*)d_ws + WSOFF);
}
```

```cpp
#include <hip/hip_runtime.h>
#include <stdint.h>

typedef short    s16x4  __attribute__((ext_vector_type(4)));
typedef __bf16   bf16x8 __attribute__((ext_vector_type(8)));
typedef __bf16   bf16x2 __attribute__((ext_vector_type(2)));
typedef float    f32x2  __attribute__((ext_vector_type(2)));
typedef float    f32x4  __attribute__((ext_vector_type(4)));
typedef unsigned u32x4  __attribute__((ext_vector_type(4)));
typedef unsigned u32x2  __attribute__((ext_vector_type(2)));
typedef unsigned u32x8  __attribute__((ext_vector_type(8)));
typedef unsigned u32x16 __attribute__((ext_vector_type(16)));
typedef unsigned long long u64;
#define LAS __attribute__((address_space(3)))
#define GAS __attribute__((address_space(1)))
#define DI __device__ __forceinline__

#define ROT ((i + (i >> 2)) & 1)
#define CODEPF 1
#define FULL 1
#define B2E 0
#define DEP 4
#define PRE 8
#define SKIP 1
#define RB 2
#define ROT2(c) 0
#define W2S 4
#define POLLW 1
#define NP 1
#define PS0 20
#define PS1 4
#define EARLY 0
#define OAUX 16
#define THR -1

constexpr unsigned UNIT = 16384;
constexpr unsigned HP = SKIP ? 2048 : 1024;
constexpr unsigned H_BYTES = 32u * 14u * 16u * HP;
constexpr unsigned FLAG_OFF = 16u << 20;
constexpr int CODE_SECT = 197;
constexpr unsigned RED_Q = 64u * 272u;
#define STAMP_OFF (32u << 20)

#define STAMP(i)

DI void glds4(unsigned ldsaddr, const char* base, const unsigned (&v)[4]) {
  asm volatile("s_mov_b32 m0, %0\n\ts_nop 0\n\t"
               "global_load_lds_dwordx4 %2, %1\n\t"
               "global_load_lds_dwordx4 %3, %1 offset:1024\n\t"
               "global_load_lds_dwordx4 %4, %1 offset:2048\n\t"
               "global_load_lds_dwordx4 %5, %1 offset:3072"
               :: "s"(__builtin_amdgcn_readfirstlane(ldsaddr)), "s"(base), "v"(v[0]), "v"(v[1]), "v"(v[2]), "v"(v[3]) : "memory");
}
DI void glds1(const char* base, unsigned v) { asm volatile("global_load_lds_dwordx4 %0, %1" :: "v"(v), "s"(base) : "memory"); }
#define VM_BAR(n) asm volatile("s_waitcnt vmcnt(%0)\n\ts_barrier" :: "i"(n) : "memory")
#define VM_WAIT(n) asm volatile("s_waitcnt vmcnt(%0)" :: "i"(n) : "memory")
#define LG_BAR do { __builtin_amdgcn_s_waitcnt(0xC07F); asm volatile("s_barrier" ::: "memory"); } while (0)
#define BAR asm volatile("s_barrier" ::: "memory")
#define SCHED __builtin_amdgcn_sched_barrier(0)

DI bf16x8 tr_frag(LAS char* p0, LAS char* p1) {
  s16x4 a = __builtin_amdgcn_ds_read_tr16_b64_v4i16((LAS s16x4*)p0);
  s16x4 b = __builtin_amdgcn_ds_read_tr16_b64_v4i16((LAS s16x4*)p1);
  return __builtin_bit_cast(bf16x8, __builtin_shufflevector(a, b, 0, 1, 2, 3, 4, 5, 6, 7));
}
DI unsigned pk2(f32x2 v) { return __builtin_bit_cast(unsigned, __builtin_convertvector(v, bf16x2)); }
DI unsigned mish2(float h0, float h1) {
  const f32x2 h = {h0, h1};
  const f32x2 t = h * 1.44269504089f + (-14.f);
  const f32x2 E = {__builtin_amdgcn_fmed3f(__builtin_amdgcn_exp2f(t.x), 0.f, 1.f), __builtin_amdgcn_fmed3f(__builtin_amdgcn_exp2f(t.y), 0.f, 1.f)};
  const f32x2 W = E + 0x1p-13f;
  const f32x2 D = E * W + 0x1p-27f;
  const f32x2 R = {__builtin_amdgcn_rcpf(D.x), __builtin_amdgcn_rcpf(D.y)};
  return pk2(h * (E * (W * R)));
}
template <int N> struct IC { static constexpr int v = N; };
template <int A, int B, class F> DI void sfor(F&& f) { if constexpr (A < B) { f(IC<A>{}); sfor<A + 1, B>(f); } }
#define MFMA(a, b, c) __builtin_amdgcn_mfma_f32_16x16x32_bf16(a, b, c, 0, 0, 0)
DI const char* ptr(unsigned lo, unsigned hi) { return (const char*)(((u64)hi << 32) | lo); }

DI constexpr int w2unit(int st) { constexpr int U[14] = {6, 7, 8, 9, 2, 5, 0, 3, 1, 4, 6, 7, 8, 9}; return U[st]; }

__global__ __launch_bounds__(768, 3) void mlp_fused(const char* __restrict__, const char* __restrict__, const char* __restrict__,
                                                    const char* __restrict__, char* __restrict__, char*) {
  __shared__ __attribute__((aligned(1024))) char smem[163840];
  LAS char* sm = (LAS char*)smem;
  const unsigned lds0 = (unsigned)(size_t)sm;
#define ARGR(id) u32x8 ka; u32x4 kb; \
  asm volatile("s_load_dwordx8 %0, %2, 0\n\ts_load_dwordx4 %1, %2, 0x20 ; " id : "=&s"(ka), "=&s"(kb) : "s"(__builtin_amdgcn_kernarg_segment_ptr())); SCHED
#define ARGW asm volatile("s_waitcnt lgkmcnt(0)" : "+s"(ka), "+s"(kb))
#define ARGS ARGW; \
  const char* x = ptr(ka[0], ka[1]); const char* w1 = ptr(ka[2], ka[3]); const char* b1 = ptr(ka[4], ka[5]); const char* w2 = ptr(ka[6], ka[7]); \
  char* out = (char*)ptr(kb[0], kb[1]); char* ws = (char*)ptr(kb[2], kb[3]); (void)x, (void)w1, (void)b1, (void)w2, (void)out
  const int w = __builtin_amdgcn_readfirstlane(threadIdx.x >> 6);
#define GEOM(id) int b = blockIdx.x, l = threadIdx.x; asm volatile("; " id : "+s"(b)); asm volatile("; " id : "+v"(l)); l &= 63; const int xcd = b & 7, i = b >> 3; \
  const int r2 = xcd * 4 + (i >> 3), c2 = i & 7, rt = (xcd >> 1) * 4 + (i & 3), ct = (-(xcd & 1) & 7) + (i >> 2)

  if (__builtin_expect(w >= 8, 1)) {
    ARGR("loader");
    GEOM("loader");
    (void)r2;
    const int j = w - 8;
    const unsigned ldsw = lds0 + j * 4096;
    unsigned vw2[4], vx[4], vw[4];
    vx[0] = (32 * j + (l >> 3)) * 1024 + (((l & 7) ^ (l >> 4)) * 16);
    vw[0] = (16 * j + (l >> 4)) * 3584 + (((l & 15) ^ ((l >> 4) << 2)) * 16);
#pragma unroll
    for (int k = 1; k < 4; k++) { vx[k] = (vx[0] ^ ((k & 1) * 64)) + k * 7168; vw[k] = (vw[0] ^ (k * 16)) + k * (4 * 3584 - 1024); }
    const bool rot = ROT;
    unsigned x0 = rt * 131072 + (rot ? 512 : 0), w0 = ct * 256 + (rot ? 256 * 3584 : 0);
    int xstep = rot ? -512 : 512, wstep = rot ? -256 * 3584 : 256 * 3584;
    asm volatile("" : "+v"(vx[0]), "+v"(vx[1]), "+v"(vx[2]), "+v"(vx[3]), "+v"(vw[0]), "+v"(vw[1]), "+v"(vw[2]), "+v"(vw[3]));
    asm volatile("" : "+s"(x0), "+s"(w0), "+s"(xstep), "+s"(wstep));
    ARGS;
    const int d2 = ROT2(c2) ? 7 * 131072 : 0;
    const char* w2c = w2 + c2 * 128;
    auto stage_w2 = [&](int st) { glds4(ldsw + w2unit(st) * UNIT, w2c + st * 131072 + (st < 7 ? d2 : -d2), vw2); };
    auto init_w2 = [&]() {
#pragma unroll
      for (int k = 0; k < 4; k++) vw2[k] = (32 * j + (l >> 3)) * 1024 + (((l & 7) ^ (((l >> 4) & 3) << 1)) * 16) + k * 7168;
    };
    if (__builtin_expect(i < 28, 1)) {
      const char* xb = x + x0;
      const char* wb = w1 + w0;
      auto stage1 = [&](int s) {
        glds4(ldsw + (s % 3) * UNIT, xb + (s & 3) * 128 + (s & 4 ? xstep : 0), vx);
        glds4(ldsw + (3 + s % 3) * UNIT, wb + (s & 3) * (64 * 3584) + (s & 4 ? wstep : 0), vw);
      };
      stage1(0);
      STAMP(15);
      if (j == 1) { u64 pc; asm volatile("s_getpc_b64 %0" : "=s"(pc));
        asm volatile("s_mov_b32 m0, %2\n\ts_mov_b64 exec, 0xff\n\tglobal_load_lds_dword %0, %1\n\ts_mov_b64 exec, -1"
                     :: "v"((unsigned)min(i * 7 + l, CODE_SECT - 1) * 64), "s"(pc), "s"(lds0 + 9 * UNIT + 4096) : "memory"); }
      BAR;
      stage1(1);
      if (B2E) stage1(2);
      init_w2();
#pragma unroll
      for (int s = 0; s < 8; s++) {
        VM_BAR(s == 0 && B2E ? 16 : 8);
        if (s < 6 && !(s == 0 && B2E)) stage1(s + 2);
        if (s == 6) { stage_w2(0); stage_w2(1); }
        if (s == 0) STAMP(2);
      }
#pragma unroll
      for (int st = 2; st < PRE; st++) stage_w2(st);
      STAMP(3);
      VM_BAR(4 * (PRE - 4));
      STAMP(5);
      BAR;
      STAMP(7);
#pragma unroll
      for (int st = PRE; st < 14; st++) { stage_w2(st); if (THR >= 0) VM_WAIT(THR >= 0 ? THR : 0); }
      STAMP(8);
    } else {
      init_w2();
      BAR;
#pragma nounroll
      for (int st = 0; st < 14; st++) {
        if (st == 4) VM_BAR(0);
        if (st == 10) BAR;
        const int u = (int)(0x98764130529876ull >> (4 * st)) & 15;
        glds4(ldsw + u * UNIT, w2c + st * 131072 + (st < 7 ? d2 : -d2), vw2);
        VM_WAIT(8);
      }
    }
    VM_BAR(RB == 2 ? 24 : 63);
    VM_BAR(0);
    STAMP(12);
    if (b == 255 && j == 0) {
      GAS u64* flags = (GAS u64*)(ws + FLAG_OFF);
      const u64 tag = __hip_atomic_load(flags + 256 * 16, __ATOMIC_RELAXED, __HIP_MEMORY_SCOPE_AGENT) + 1;
#pragma nounroll
      for (int spins = 0; spins < (1 << 18); spins++) {
        u64 v[4];
#pragma unroll
        for (int k = 0; k < 4; k++) v[k] = __hip_atomic_load(flags + (k * 64 + l) * 16, __ATOMIC_RELAXED, __HIP_MEMORY_SCOPE_AGENT);
        if (__all(v[0] == tag && v[1] == tag && v[2] == tag && v[3] == tag)) break;
      }
      if (l == 0) __hip_atomic_store(flags + 256 * 16, tag, __ATOMIC_RELAXED, __HIP_MEMORY_SCOPE_AGENT);
    }
    return;
  }

  ARGR("compute");
  GEOM("compute");
  const int g = l >> 4, q4 = (l & 15) >> 2, p = l & 3;
  const unsigned hl = SKIP ? l * 16 + (l >= 24 ? 128 : 0) : l * 16;
  ARGS;
  STAMP(13);
  GAS u64* flags = (GAS u64*)(ws + FLAG_OFF);
  const __amdgpu_buffer_rsrc_t hrs = __builtin_amdgcn_make_buffer_rsrc(ws, 0, H_BYTES, 0x00020000);
  u64 tag = *(const __attribute__((address_space(4))) u64*)(size_t)(ws + FLAG_OFF + 256 * 128);

  if (__builtin_expect(i < 28, 1)) {
    const int fq = w & 3, mh = w >> 2;
    u32x2 bb[2];
#pragma unroll
    for (int fb = 0; fb < 2; fb++) bb[fb] = *(const GAS u32x2*)(b1 + (ct * 128 + fq * 32 + fb * 16 + g * 4) * 2);
    SCHED;
    LAS char* xa[2];
#pragma unroll
    for (int ks = 0; ks < 2; ks++) xa[ks] = sm + (64 * mh + (l & 15)) * 128 + (((4 * ks + g) ^ ((l >> 1) & 7)) * 16);
    LAS char* wa[2][2];
#pragma unroll
    for (int fb = 0; fb < 2; fb++)
#pragma unroll
      for (int t = 0; t < 2; t++) {
        unsigned o = 3 * UNIT + (8 * g + 4 * t + q4) * 256 + (((4 * fq + 2 * fb + (p >> 1)) ^ ((q4 << 2) | ((2 * g + t) & 3))) * 16) + 8 * (p & 1);
        asm("" : "+v"(o));
        wa[fb][t] = sm + o;
      }
    const unsigned hv = hl + (((rt * 2 + mh) * 14 + ct) * 4 + fq) * 4 * HP;
    SCHED;
    BAR;
    STAMP(1);
    asm volatile("" : "+s"(tag));
    tag += 1;
    f32x4 acc[2][4];
    auto init = [&]() {
#pragma unroll
      for (int fb = 0; fb < 2; fb++)
#pragma unroll
        for (int m = 0; m < 4; m++)
          acc[fb][m] = (f32x4){__uint_as_float(bb[fb][0] << 16), __uint_as_float(bb[fb][0] & 0xffff0000u), __uint_as_float(bb[fb][1] << 16), __uint_as_float(bb[fb][1] & 0xffff0000u)};
      SCHED;
    };
    auto act = [&](int m) {
      const u32x4 o = {mish2(acc[0][m][0], acc[0][m][1]), mish2(acc[0][m][2], acc[0][m][3]), mish2(acc[1][m][0], acc[1][m][1]), mish2(acc[1][m][2], acc[1][m][3])};
      __builtin_amdgcn_raw_buffer_store_b128(o, hrs, hv + m * HP, 0, 16);
      SCHED;
    };
    bf16x8 a[2][2][2], bx[2][2][4];
    sfor<0, 8>([&](auto S) {
      constexpr int s = S.v, c = s & 1;
      SCHED;
      LG_BAR;
      if (s == 0) STAMP(2);
      if (s == 7) STAMP(3);
      constexpr unsigned so = (s % 3) * UNIT;
      auto rda = [&](int ks, int fb) { a[c][ks][fb] = tr_frag(wa[fb][0] + so + ks * 8192, wa[fb][1] + so + ks * 8192); SCHED; };
      auto rdb = [&](int ks, int m) { bx[c][ks][m] = *(LAS bf16x8*)(xa[ks] + so + m * 2048); SCHED; };
      auto mm = [&](int n) { const int ks = n >> 3, m = (n >> 1) & 3, fb = n & 1; if (s > 0) { acc[fb][m] = MFMA(a[c ^ 1][ks][fb], bx[c ^ 1][ks][m], acc[fb][m]); SCHED; } };
      if constexpr (s < 7 || !EARLY) {
        mm(0); rda(0, 0); mm(1); rdb(0, 0); mm(2); rda(0, 1); mm(3); rdb(0, 1); mm(4); rda(1, 0); mm(5); rdb(1, 0); mm(6); rda(1, 1); mm(7); rdb(1, 1);
        mm(8); rdb(0, 2); mm(9); rdb(1, 2); mm(10); rdb(0, 3); mm(11); rdb(1, 3); mm(12); mm(13); mm(14); mm(15);
      } else {
        mm(0); rda(0, 0); mm(1); rda(0, 1); mm(8); rdb(0, 0); mm(9); rda(1, 0); mm(2); rda(1, 1); mm(3); rdb(1, 0); mm(10); rdb(0, 1); mm(11); rdb(1, 1);
        rdb(0, 2); rdb(1, 2); rdb(0, 3); rdb(1, 3);
      }
      if (s == 0) init();
    });
    auto prod = [&](int c, int m) {
#pragma unroll
      for (int ks = 0; ks < 2; ks++)
#pragma unroll
        for (int fb = 0; fb < 2; fb++) acc[fb][m] = MFMA(a[c][ks][fb], bx[c][ks][m], acc[fb][m]);
      if (EARLY) SCHED;
    };
    prod(1, 0); prod(1, 1);
    SCHED;
    STAMP(4);
#pragma unroll
    for (int m = 0; m < 4; m++) { if (m < 2) prod(1, 2 + m); act(m); }
    STAMP(5);
  } else {
    const unsigned e = (unsigned)(w * 64 + l), hh = i - 28;
    asm volatile("s_mov_b32 m0, %0" :: "s"(lds0 + w * 1024) : "memory");
    const unsigned vx = ((xcd >> 1) * 512 + (xcd & 1) * 256 + hh * 64 + w * 8 + (l >> 3)) * 1024 + (l & 7) * 16;
    const unsigned v1 = ((xcd >> 1) * 16 + hh * 4 + e / 112) * 3584 + (xcd & 1) * 1792 + (e % 112) * 16;
#pragma unroll
    for (int n = 0; n < 4; n++) {
      const int k = 2 + (n >> 1) + (n & 1) * 4;
      glds1(x + k * 128, vx);
      if (w < 7) glds1(w1 + k * 64 * 3584, v1);
    }
    const unsigned cs = (3 - (unsigned)((size_t)w2 >> 7)) & 7;
    const unsigned v2 = ((w >> 2) * 128 + xcd * 16 + hh * 4 + (w & 3)) * 1024 + l * 16;
    const unsigned v3 = ((w >> 2) * 128 + hh * 32 + (w & 3) * 8 + (l >> 3)) * 1024 + cs * 128 + (l & 7) * 16;
#pragma unroll
    for (int h = 0; h < 2; h++) {
#pragma unroll
      for (int k = h * W2S; k < (h ? 7 : W2S); k++) glds1(w2 + k * 262144, v2);
#pragma unroll
      for (int k = h * W2S; k < (h ? 7 : W2S); k++) glds1(w2 + (ROT2(cs) ? (k + 3) % 7 : k) * 262144, v3);
    }
    BAR;
    STAMP(1);
    asm volatile("" : "+s"(tag));
    tag += 1;
    VM_WAIT(0);
  }
  SCHED;
  BAR;
  {
    const int q = w & 3, mb = w >> 2;
    LAS char* wa[3][4];
#pragma unroll
    for (int k = 0; k < 3; k++)
#pragma unroll
      for (int c = 0; c < 4; c++) {
        unsigned o = k * 65536 + (32 * q + 4 * g + q4) * 128 + (((2 * c + (p >> 1)) ^ ((((2 * g) | (q4 >> 1)) & 3) << 1)) * 16) + 8 * (p & 1);
        asm("" : "+v"(o));
        wa[k][c] = sm + o;
      }
    bf16x8 af[14][4];
    auto read_w = [&](int st, int c) {
      const int u = w2unit(st);
      LAS char* a = wa[u >> 2][c] + (u & 3) * UNIT;
      af[st][c] = tr_frag(a, a + 2048);
    };
#pragma unroll
    for (int st = 0; st < 4; st++)
#pragma unroll
      for (int c = 0; c < 4; c++) read_w(st, c);
    SCHED;
    VM_WAIT(0);
    STAMP(6);
    LG_BAR;
    const int hid = xcd * 4 + (i - 28), slot = i < 28 ? rt * 16 + ct : (hid >> 1) * 16 + 14 + (hid & 1);
    if (w == 0 && l == 0) __hip_atomic_store(flags + slot * 16, tag, __ATOMIC_RELAXED, __HIP_MEMORY_SCOPE_AGENT);
    STAMP(15);
    if (w == POLLW) {
      GAS u64* fp = flags + ((r2 >> 1) * 16 + (l < 14 ? l : 0)) * 16;
      if (PS0) __builtin_amdgcn_s_sleep(PS0);
#pragma nounroll
      for (int spins = 0; spins < (1 << 18); spins++)
        if (__all(__hip_atomic_load(fp, __ATOMIC_RELAXED, __HIP_MEMORY_SCOPE_AGENT) == tag)) break;
      STAMP(14);
    }
    SCHED;
    BAR;
    STAMP(7);
    const unsigned hv = hl + (q * 2 + mb) * 2 * HP;
    const unsigned hs = r2 * 14 * 16 * HP, dh = ROT2(c2) ? 7 * 16 * HP : 0;
    u32x4 hf[14][2];
    auto load_h = [&](int st) {
      const unsigned so = hs + st * 16 * HP + (st < 7 ? dh : -dh);
      hf[st][0] = __builtin_amdgcn_raw_buffer_load_b128(hrs, hv, so, 16);
      hf[st][1] = __builtin_amdgcn_raw_buffer_load_b128(hrs, hv + HP, so, 16);
    };
#pragma unroll
    for (int st = 0; st < DEP; st++) load_h(st);
    SCHED;
    STAMP(8);
    f32x4 acc[4][2] = {};
#pragma unroll
    for (int st = 0; st < 14; st++) {
      SCHED;
      if (st == 3 && RB != 2) BAR;
      if (st == 7 && RB == 2) LG_BAR;
      if (st == 9) STAMP(9);
      if (st == 10 && RB == 1) BAR;
#pragma unroll
      for (int m = 0; m < 2; m++)
#pragma unroll
        for (int c = 0; c < 4; c++) {
          acc[c][m] = MFMA(af[st][c], __builtin_bit_cast(bf16x8, hf[st][m]), acc[c][m]);
          SCHED;
          if (st >= 3 && st + 1 < 14 && m == 0) { read_w(st + 1, c); SCHED; }
        }
      if (st + DEP < 14) load_h(st + DEP);
    }
    SCHED;
    STAMP(10);
    const unsigned ro = ((0x3052 >> (4 * q)) & 15) * UNIT + (32 * mb + (l & 15)) * 256 + ((g ^ (l & 7)) * 16);
#pragma unroll
    for (int m = 0; m < 2; m++)
#pragma unroll
      for (int c = 0; c < 4; c++) *(LAS f32x4*)(sm + (ro ^ ((c & 1) * 64)) + m * 4096 + (c >> 1) * 128) = acc[c][m];
    LG_BAR;
    const unsigned rr = (8 * w + (l >> 3)) * 256 + (((2 * (l & 7)) ^ (l >> 3)) * 16);
    f32x4 s0 = *(LAS f32x4*)(sm + rr), s1 = *(LAS f32x4*)(sm + (rr ^ 16));
#pragma unroll
    for (int k = 1; k < 4; k++) { const unsigned u = (k == 1 ? 2 : k == 2 ? 3 : 5) * UNIT; s0 += *(LAS f32x4*)(sm + rr + u); s1 += *(LAS f32x4*)(sm + (rr ^ 16) + u); }
    const u32x4 o = {pk2((f32x2){s0[0], s0[1]}), pk2((f32x2){s0[2], s0[3]}), pk2((f32x2){s1[0], s1[1]}), pk2((f32x2){s1[2], s1[3]})};
    __builtin_amdgcn_raw_buffer_store_b128(o, __builtin_amdgcn_make_buffer_rsrc(out, 0, 2048 * 1024, 0x00020000), (r2 * 64 + 8 * w + (l >> 3)) * 1024 + c2 * 128 + (l & 7) * 16, 0, OAUX);
  }
}

extern "C" void kernel_launch(void* const* d_in, const int* in_sizes, int n_in, void* d_out, int out_size,
                              void* d_ws, size_t ws_size, hipStream_t stream) {
  mlp_fused<<<256, 768, 0, stream>>>((const char*)d_in[0], (const char*)d_in[1], (const char*)d_in[2],
                                     (const char*)d_in[3], (char*)d_out, (char*)d_ws);
}
```

```cpp
#include <hip/hip_runtime.h>
#include <stdint.h>

typedef short    s16x4  __attribute__((ext_vector_type(4)));
typedef __bf16   bf16x8 __attribute__((ext_vector_type(8)));
typedef __bf16   bf16x2 __attribute__((ext_vector_type(2)));
typedef float    f32x2  __attribute__((ext_vector_type(2)));
typedef float    f32x4  __attribute__((ext_vector_type(4)));
typedef unsigned u32x4  __attribute__((ext_vector_type(4)));
typedef unsigned u32x2  __attribute__((ext_vector_type(2)));
typedef unsigned u32x8  __attribute__((ext_vector_type(8)));
typedef unsigned long long u64;
#define LAS __attribute__((address_space(3)))
#define GAS __attribute__((address_space(1)))
#define DI __device__ __forceinline__

#define ROTX (((xcd >> 1) + 2 * (xcd & 1)) & 3)
#define G1N 0
#define HS 10
#define DEP 4
#define PRE 8
#define W2S 4
#define PS0 20
#define PSH 20
#define OWNX 0
#define KBAR 8
#define LAG 8
#define BP 0
#define POLLW 1
#define SKIP 1
#define OAUX 16
#define OWN 0
#define KPF 2
#define BUD 12

constexpr unsigned UNIT = 16384;
constexpr unsigned HP = SKIP ? 2048 : 1024;
constexpr unsigned H_BYTES = 32u * 14u * 16u * HP;
constexpr unsigned FLAG_OFF = 16u << 20;
constexpr int CODE_SECT = 197;
#define STAMP_OFF (32u << 20)

#define STAMP(i)

DI void glds4(unsigned ldsaddr, const char* base, const unsigned (&v)[4]) {
  asm volatile("s_mov_b32 m0, %0\n\ts_nop 0\n\t"
               "global_load_lds_dwordx4 %2, %1\n\t"
               "global_load_lds_dwordx4 %3, %1 offset:1024\n\t"
               "global_load_lds_dwordx4 %4, %1 offset:2048\n\t"
               "global_load_lds_dwordx4 %5, %1 offset:3072"
               :: "s"(__builtin_amdgcn_readfirstlane(ldsaddr)), "s"(base), "v"(v[0]), "v"(v[1]), "v"(v[2]), "v"(v[3]) : "memory");
}
DI void glds1(const char* base, unsigned v) { asm volatile("global_load_lds_dwordx4 %0, %1" :: "v"(v), "s"(base) : "memory"); }
#define VM_BAR(n) asm volatile("s_waitcnt vmcnt(%0)\n\ts_barrier" :: "i"(n) : "memory")
#define VM_WAIT(n) asm volatile("s_waitcnt vmcnt(%0)" :: "i"(n) : "memory")
#define LG_BAR do { __builtin_amdgcn_s_waitcnt(0xC07F); asm volatile("s_barrier" ::: "memory"); } while (0)
#define BAR asm volatile("s_barrier" ::: "memory")
#define SCHED __builtin_amdgcn_sched_barrier(0)

DI bf16x8 tr_frag(LAS char* p0, LAS char* p1) {
  s16x4 a = __builtin_amdgcn_ds_read_tr16_b64_v4i16((LAS s16x4*)p0);
  s16x4 b = __builtin_amdgcn_ds_read_tr16_b64_v4i16((LAS s16x4*)p1);
  return __builtin_bit_cast(bf16x8, __builtin_shufflevector(a, b, 0, 1, 2, 3, 4, 5, 6, 7));
}
DI unsigned pk2(f32x2 v) { return __builtin_bit_cast(unsigned, __builtin_convertvector(v, bf16x2)); }
DI unsigned mish2(float h0, float h1) {
  const f32x2 h = {h0, h1};
  const f32x2 t = h * 1.44269504089f + (-14.f);
  const f32x2 E = {__builtin_amdgcn_fmed3f(__builtin_amdgcn_exp2f(t.x), 0.f, 1.f), __builtin_amdgcn_fmed3f(__builtin_amdgcn_exp2f(t.y), 0.f, 1.f)};
  const f32x2 W = E + 0x1p-13f;
  const f32x2 D = E * W + 0x1p-27f;
  const f32x2 R = {__builtin_amdgcn_rcpf(D.x), __builtin_amdgcn_rcpf(D.y)};
  return pk2(h * (E * (W * R)));
}
template <int N> struct IC { static constexpr int v = N; };
template <int A, int B, class F> DI void sfor(F&& f) { if constexpr (A < B) { f(IC<A>{}); sfor<A + 1, B>(f); } }
constexpr int act_first(int cycles) {
  int n = 0;
  for (int c = 0; n < 2 * 34 && c < cycles; n++) c += (n % 34) >> 2 == 1 || (n % 34) >> 2 == 4 ? 8 : 4;
  return n;
}
#define MFMA(a, b, c) __builtin_amdgcn_mfma_f32_16x16x32_bf16(a, b, c, 0, 0, 0)
DI const char* ptr(unsigned lo, unsigned hi) { return (const char*)(((u64)hi << 32) | lo); }

DI constexpr int w2unit(int st) { constexpr int U[14] = {6, 7, 8, 9, 2, 5, 0, 3, 1, 4, 6, 7, 8, 9}; return U[st]; }

__global__ __launch_bounds__(768, 3) void mlp_fused(const char* __restrict__, const char* __restrict__, const char* __restrict__,
                                                    const char* __restrict__, char* __restrict__, char*) {
  __shared__ __attribute__((aligned(1024))) char smem[163840];
  LAS char* sm = (LAS char*)smem;
  const unsigned lds0 = (unsigned)(size_t)sm;
  asm volatile("v_readfirstlane_b32 s95, %1\n\ts_cmp_lg_u32 s95, 0\n\ts_cbranch_scc1 1f\n\ts_load_dword s95, %0, 0\n1:" :: "s"(__builtin_amdgcn_kernarg_segment_ptr()), "v"(threadIdx.x) : "s95", "scc");
#define ARGR(id) u32x8 ka; u32x4 kb; \
  asm volatile("s_load_dwordx8 %0, %2, 0\n\ts_load_dwordx4 %1, %2, 0x20 ; " id : "=&s"(ka), "=&s"(kb) : "s"(__builtin_amdgcn_kernarg_segment_ptr())); SCHED
#define ARGW asm volatile("s_waitcnt lgkmcnt(0)" : "+s"(ka), "+s"(kb))
#define ARGS ARGW; \
  const char* x = ptr(ka[0], ka[1]); const char* w1 = ptr(ka[2], ka[3]); const char* b1 = ptr(ka[4], ka[5]); const char* w2 = ptr(ka[6], ka[7]); \
  char* out = (char*)ptr(kb[0], kb[1]); char* ws = (char*)ptr(kb[2], kb[3]); (void)x, (void)w1, (void)b1, (void)w2, (void)out
  const int w = __builtin_amdgcn_readfirstlane(threadIdx.x >> 6);
#define GEOM2 r2 = xcd * 4 + (i >> 3), c2 = i & 7; auto blk = [&](int st) { return st; }
#define GEOM(id) int b = blockIdx.x, l = threadIdx.x; asm volatile("; " id : "+s"(b)); asm volatile("; " id : "+v"(l)); l &= 63; const int xcd = b & 7, i = b >> 3; \
  const int rt = (xcd >> 1) * 4 + (i & 3), ct = (-(xcd & 1) & 7) + (i >> 2), GEOM2

  if (__builtin_expect(w >= 8, 1)) {
    ARGR("loader");
    GEOM("loader");
    (void)r2;
    const int j = w - 8;
    const unsigned ldsw = lds0 + j * 4096;
    unsigned vw2[4], vx[4], vw[4];
    vx[0] = (32 * j + (l >> 3)) * 1024 + (((l & 7) ^ (l >> 4)) * 16);
    vw[0] = (16 * j + (l >> 4)) * 3584 + (((l & 15) ^ ((l >> 4) << 2)) * 16);
#pragma unroll
    for (int k = 1; k < 4; k++) { vx[k] = (vx[0] ^ ((k & 1) * 64)) + k * 7168; vw[k] = (vw[0] ^ (k * 16)) + k * (4 * 3584 - 1024); }
    int rot = 4 * ((i + (i >> 2)) & 1) + (ROTX);
    unsigned x0 = rt * 131072 + rot * 128, w0 = ct * 256 + rot * (64 * 3584);
    asm volatile("" : "+v"(vx[0]), "+v"(vx[1]), "+v"(vx[2]), "+v"(vx[3]), "+v"(vw[0]), "+v"(vw[1]), "+v"(vw[2]), "+v"(vw[3]));
    asm volatile("" : "+s"(x0), "+s"(w0), "+s"(rot));
    ARGS;
    const char* w2c = w2 + c2 * 128;
    auto stage_w2 = [&](int st) { glds4(ldsw + w2unit(st) * UNIT, w2c + blk(st) * 131072, vw2); };
    auto init_w2 = [&]() {
#pragma unroll
      for (int k = 0; k < 4; k++) vw2[k] = (32 * j + (l >> 3)) * 1024 + (((l & 7) ^ (((l >> 4) & 3) << 1)) * 16) + k * 7168;
    };
    if (__builtin_expect(i < 28, 1)) {
      const char* xb = x + x0;
      const char* wb = w1 + w0;
      auto stage1 = [&](int s) {
        const int k = s == 0 || s + rot < 8 ? s : s - 8;
        glds4(ldsw + (s % 3) * UNIT, xb + k * 128, vx);
        glds4(ldsw + (3 + s % 3) * UNIT, wb + k * (64 * 3584), vw);
      };
      stage1(0);
      STAMP(15);
      if (j == 1) { u64 pc; asm volatile("s_getpc_b64 %0" : "=s"(pc));
        asm volatile("s_mov_b32 m0, %2\n\ts_mov_b64 exec, 0xff\n\tglobal_load_lds_dword %0, %1\n\ts_mov_b64 exec, -1"
                     :: "v"((unsigned)min(i * 7 + l, CODE_SECT - 1) * 64), "s"(pc), "s"(lds0 + 9 * UNIT + 4096) : "memory"); }
      BAR;
      stage1(1);
      init_w2();
#pragma unroll
      for (int s = 0; s < 8; s++) {
        VM_BAR(8);
        if (s < 6) stage1(s + 2);
        if (s == 6) { stage_w2(0); stage_w2(1); }
        if (s == 0) STAMP(2);
      }
#pragma unroll
      for (int st = 2; st < PRE; st++) { if (st == 8) BAR; stage_w2(st); }
      STAMP(3);
      VM_BAR(4 * (PRE - 4));
      STAMP(5);
      if (OWN) BAR;
      BAR;
      STAMP(7);
#pragma unroll
      for (int st = PRE; st < 14; st++) stage_w2(st);
      STAMP(8);
    } else {
      init_w2();
      BAR;
#pragma nounroll
      for (int st = 0; st < 14; st++) {
        if (st == 4) VM_BAR(0);
        if (st == 10) BAR;
        const int u = (int)(0x98764130529876ull >> (4 * st)) & 15;
        glds4(ldsw + u * UNIT, w2c + blk(st) * 131072, vw2);
        VM_WAIT(8);
      }
    }
    VM_BAR(24);
    VM_BAR(0);
    STAMP(12);
    if (b == 255 && j == 0) {
      GAS u64* flags = (GAS u64*)(ws + FLAG_OFF);
      const u64 tag = __hip_atomic_load(flags + 256 * 16, __ATOMIC_RELAXED, __HIP_MEMORY_SCOPE_AGENT) + 1;
#pragma nounroll
      for (int spins = 0; spins < (1 << 18); spins++) {
        u64 v[4];
#pragma unroll
        for (int k = 0; k < 4; k++) v[k] = __hip_atomic_load(flags + (k * 64 + l) * 16, __ATOMIC_RELAXED, __HIP_MEMORY_SCOPE_AGENT);
        if (__all(v[0] == tag && v[1] == tag && v[2] == tag && v[3] == tag)) break;
      }
      if (l == 0) __hip_atomic_store(flags + 256 * 16, tag, __ATOMIC_RELAXED, __HIP_MEMORY_SCOPE_AGENT);
    }
    return;
  }

  ARGR("compute");
  GEOM("compute");
  const int g = l >> 4, q4 = (l & 15) >> 2, p = l & 3;
  const unsigned hl = l * 16 + (SKIP == 1 && l >= 24 ? 128 : 0) + (SKIP == 2 ? 512 : 0);
  ARGS;
  STAMP(13);
  GAS u64* flags = (GAS u64*)(ws + FLAG_OFF);
  const __amdgpu_buffer_rsrc_t hrs = __builtin_amdgcn_make_buffer_rsrc(ws, 0, H_BYTES, 0x00020000);
  u64 tag = *(const __attribute__((address_space(4))) u64*)(size_t)(ws + FLAG_OFF + 256 * 128);
  u32x4 ho[4];

  if (__builtin_expect(i < 28, 1)) {
    const int fq = w & 3, mh = w >> 2;
    u32x2 bb[2];
#pragma unroll
    for (int fb = 0; fb < 2; fb++) if (!BP) bb[fb] = *(const GAS u32x2*)(b1 + (ct * 128 + fq * 32 + fb * 16 + g * 4) * 2);
    SCHED;
    LAS char* xa[2];
#pragma unroll
    for (int ks = 0; ks < 2; ks++) xa[ks] = sm + (64 * mh + (l & 15)) * 128 + (((4 * ks + g) ^ ((l >> 1) & 7)) * 16);
    LAS char* wa[2][2];
#pragma unroll
    for (int fb = 0; fb < 2; fb++)
#pragma unroll
      for (int t = 0; t < 2; t++) {
        unsigned o = 3 * UNIT + (8 * g + 4 * t + q4) * 256 + (((4 * fq + 2 * fb + (p >> 1)) ^ ((q4 << 2) | ((2 * g + t) & 3))) * 16) + 8 * (p & 1);
        asm("" : "+v"(o));
        wa[fb][t] = sm + o;
      }
    const unsigned hv = hl + (((rt * 2 + mh) * 14 + ct) * 4 + fq) * 4 * HP;
    SCHED;
    BAR;
#pragma unroll
    for (int fb = 0; fb < 2; fb++) if (BP) bb[fb] = *(const GAS u32x2*)(b1 + (ct * 128 + fq * 32 + fb * 16 + g * 4) * 2);
    SCHED;
    STAMP(1);
    asm volatile("" : "+s"(tag));
    tag += 1;
    f32x4 acc[2][4];
    auto put = [&](int m) { __builtin_amdgcn_raw_buffer_store_b128(ho[m], hrs, hv + m * HP, 0, 16); };
    auto act = [&](int m) {
      ho[m] = (u32x4){mish2(acc[0][m][0], acc[0][m][1]), mish2(acc[0][m][2], acc[0][m][3]), mish2(acc[1][m][0], acc[1][m][1]), mish2(acc[1][m][2], acc[1][m][3])};
      put(m);
      SCHED;
    };
    bf16x8 a[2][2][2], bx[2][2][4];
    sfor<0, 8>([&](auto S) {
      constexpr int s = S.v, c = s & 1;
      SCHED;
      LG_BAR;
      if (s == 0) STAMP(2);
      if (s == 7) STAMP(3);
      constexpr unsigned so = (s % 3) * UNIT;
      auto rda = [&](int ks, int fb) { a[c][ks][fb] = tr_frag(wa[fb][0] + so + ks * 8192, wa[fb][1] + so + ks * 8192); SCHED; };
      auto rdb = [&](int ks, int m) { bx[c][ks][m] = *(LAS bf16x8*)(xa[ks] + so + m * 2048); SCHED; };
      auto mm = [&](int n) { const int ks = n >> 3, m = (n >> 1) & 3, fb = n & 1; if (s > 0) { acc[fb][m] = MFMA(a[c ^ 1][ks][fb], bx[c ^ 1][ks][m], acc[fb][m]); SCHED; } };
      auto init = [&]() {
#pragma unroll
        for (int fb = 0; fb < 2; fb++)
#pragma unroll
          for (int m = 0; m < 4; m++)
            acc[fb][m] = (f32x4){__uint_as_float(bb[fb][0] << 16), __uint_as_float(bb[fb][0] & 0xffff0000u), __uint_as_float(bb[fb][1] << 16), __uint_as_float(bb[fb][1] & 0xffff0000u)};
        SCHED;
      };
      auto mc = [&](int n) { const int m = (n >> 1) & 3, fb = n & 1; acc[fb][m] = MFMA(a[c][0][fb], bx[c][0][m], acc[fb][m]); SCHED; };
      if constexpr (s < 7) {
        mm(8); rda(0, 0); rdb(0, 0); mm(9); rda(0, 1); mm(10); rdb(0, 1); mm(11); rdb(0, 2); mm(12); rdb(0, 3); mm(13); rda(1, 0); mm(14); rdb(1, 0); mm(15); rda(1, 1);
        if (s == 0) init();
        mc(0); rdb(1, 1); mc(1); rdb(1, 2); mc(2); rdb(1, 3); mc(3); mc(4); mc(5); mc(6); mc(7);
      } else {
        mm(8); rda(0, 0); rdb(0, 0); mm(9); rda(0, 1); mm(10); rda(1, 0); mm(11); rdb(1, 0); mm(12); rda(1, 1); mm(13); rdb(0, 1); mm(14); rdb(1, 1); mm(15);
        rdb(0, 2); rdb(1, 2); rdb(0, 3); rdb(1, 3);
      }
    });
    auto prod1 = [&](int c, int m, int n) { acc[n & 1][m] = MFMA(a[c][n >> 1][n & 1], bx[c][n >> 1][m], acc[n & 1][m]); };
    auto prod = [&](int c, int m) { prod1(c, m, 0); prod1(c, m, 1); prod1(c, m, 2); prod1(c, m, 3); };
    prod(1, 0);
    SCHED;
    STAMP(4);
    constexpr int KB = PRE > 8 ? KBAR : -1;
    float tE[4], tW[4], tD[4], tU[4];
    auto piece = [&](auto N) {
      constexpr int n = N.v, fb = n / 34, r = n % 34, op = r >> 2, e = r & 3;
      const float h = acc[fb][0][e];
      if constexpr (op == 0) tE[e] = __builtin_fmaf(h, 1.44269504089f, -14.f);
      if constexpr (op == 1) tE[e] = __builtin_amdgcn_fmed3f(__builtin_amdgcn_exp2f(tE[e]), 0.f, 1.f);
      if constexpr (op == 2) tW[e] = tE[e] + 0x1p-13f;
      if constexpr (op == 3) tD[e] = __builtin_fmaf(tE[e], tW[e], 0x1p-27f);
      if constexpr (op == 4) tD[e] = __builtin_amdgcn_rcpf(tD[e]);
      if constexpr (op == 5) tW[e] *= tD[e];
      if constexpr (op == 6) tE[e] *= tW[e];
      if constexpr (op == 7) { tU[e] = h * tE[e]; asm("" : "+v"(tU[e])); }
      if constexpr (op == 8) ho[0][fb * 2 + e] = pk2((f32x2){tU[2 * e], tU[2 * e + 1]});
    };
    constexpr int NG = LAG == 8 ? 12 : 20;
    sfor<0, NG>([&](auto K) {
      constexpr int k = K.v, grp = k >> 2;
      if (k == KB) LG_BAR;
      if (LAG == 8) prod1(1, grp + 1, k & 3); else prod1(grp == 1 || grp == 3 ? 0 : 1, (grp + 3) >> 1, k & 3);
      SCHED;
      sfor<act_first(k * BUD), act_first(k == NG - 1 ? 1000 : k * BUD + BUD)>(piece);
      SCHED;
    });
    put(0);
    SCHED;
    act(1); act(2); act(3);
    STAMP(5);
  } else {
    const unsigned e = (unsigned)(w * 64 + l), hh = i - 28;
    asm volatile("s_mov_b32 m0, %0" :: "s"(lds0 + w * 1024) : "memory");
    if (HS) __builtin_amdgcn_s_sleep(HS);
    const unsigned vx = ((xcd >> 1) * 512 + (xcd & 1) * 256 + hh * 64 + w * 8 + (l >> 3)) * 1024 + (l & 7) * 16;
    const unsigned v1 = ((xcd >> 1) * 16 + hh * 4 + e / 112) * 3584 + (xcd & 1) * 1792 + (e % 112) * 16;
#pragma unroll
    for (int n = 0; n < G1N; n++) {
      const int k = 2 + (n >> 1) + (n & 1) * 4;
      glds1(x + k * 128, vx);
      if (w < 7) glds1(w1 + k * 64 * 3584, v1);
    }
    const unsigned cs = (3 - (unsigned)((size_t)w2 >> 7)) & 7;
    const unsigned v2 = ((w >> 2) * 128 + xcd * 16 + hh * 4 + (w & 3)) * 1024 + l * 16;
    const unsigned v3 = ((w >> 2) * 128 + hh * 32 + (w & 3) * 8 + (l >> 3)) * 1024 + cs * 128 + (l & 7) * 16;
#pragma unroll
    for (int h = 0; h < 2; h++) {
#pragma unroll
      for (int k = h * W2S; k < (h ? 7 : W2S); k++) glds1(w2 + k * 262144, v2);
#pragma unroll
      for (int k = h * W2S; k < (h ? 7 : W2S); k++) glds1(w2 + k * 262144, v3);
    }
    BAR;
    STAMP(1);
    asm volatile("" : "+s"(tag));
    tag += 1;
    VM_WAIT(0);
#pragma unroll
    for (int m = 0; m < 4; m++) asm volatile("" : "=v"(ho[m]));
  }
  SCHED;
  BAR;
  {
    const int q = w & 3, mb = w >> 2;
    LAS char* ex = sm + UNIT + q * 4096 + l * 16;
    if (OWN && i < 28 && mb == (xcd & 1)) {
#pragma unroll
      for (int m = 0; m < 4; m++) *(LAS u32x4*)(ex + m * 1024) = ho[m];
    }
    SCHED;
    LAS char* wa[3][4];
#pragma unroll
    for (int k = 0; k < 3; k++)
#pragma unroll
      for (int c = 0; c < 4; c++) {
        unsigned o = k * 65536 + (32 * q + 4 * g + q4) * 128 + (((2 * c + (p >> 1)) ^ ((((2 * g) | (q4 >> 1)) & 3) << 1)) * 16) + 8 * (p & 1);
        asm("" : "+v"(o));
        wa[k][c] = sm + o;
      }
    bf16x8 af[14][4];
    auto read_w = [&](int st, int c) {
      const int u = w2unit(st);
      LAS char* a = wa[u >> 2][c] + (u & 3) * UNIT;
      af[st][c] = tr_frag(a, a + 2048);
    };
#pragma unroll
    for (int st = 0; st < 4; st++)
#pragma unroll
      for (int c = 0; c < 4; c++) read_w(st, c);
    SCHED;
    u32x4 hf[14][2];
    if (OWN) {
      if (i < 28) LG_BAR;
      hf[0][0] = *(LAS u32x4*)(ex + mb * 2048);
      hf[0][1] = *(LAS u32x4*)(ex + mb * 2048 + 1024);
    }
    VM_WAIT(0);
    STAMP(6);
    LG_BAR;
    const int hid = xcd * 4 + (i - 28), slot = i < 28 ? rt * 16 + ct : (hid >> 1) * 16 + 14 + (hid & 1);
    if (w == 0 && l == 0) __hip_atomic_store(flags + slot * 16, tag, __ATOMIC_RELAXED, __HIP_MEMORY_SCOPE_AGENT);
    STAMP(15);
    f32x4 acc[4][2] = {};
    if (OWNX && i < 28) {
#pragma unroll
      for (int m = 0; m < 2; m++)
#pragma unroll
        for (int c = 0; c < 4; c++) acc[c][m] = MFMA(af[0][c], __builtin_bit_cast(bf16x8, hf[0][m]), acc[c][m]);
    }
    SCHED;
    if (w == POLLW) {
      if (POLLW == 0) VM_WAIT(0);
      GAS u64* fp = flags + ((r2 >> 1) * 16 + (l < 14 ? l : 0)) * 16;
      if (i >= 28 && PSH) __builtin_amdgcn_s_sleep(PSH);
      __builtin_amdgcn_s_sleep(PS0);
#pragma nounroll
      for (int spins = 0; spins < (1 << 18); spins++)
        if (__all(__hip_atomic_load(fp, __ATOMIC_RELAXED, __HIP_MEMORY_SCOPE_AGENT) == tag)) break;
      STAMP(14);
    }
    SCHED;
    BAR;
    STAMP(7);
    const unsigned hv = hl + (q * 2 + mb) * 2 * HP;
    const unsigned hs = r2 * 14 * 16 * HP;
    auto load_h = [&](int st) {
      const unsigned so = hs + blk(st) * 16 * HP;
      hf[st][0] = __builtin_amdgcn_raw_buffer_load_b128(hrs, hv, so, 16);
      hf[st][1] = __builtin_amdgcn_raw_buffer_load_b128(hrs, hv + HP, so, 16);
    };
    if (!OWN || i >= 28) load_h(0);
#pragma unroll
    for (int st = 1; st < DEP + OWN; st++) load_h(st);
    SCHED;
    STAMP(8);
#pragma unroll
    for (int st = 0; st < 14; st++) {
      SCHED;
      if (OWNX && st == 0 && i < 28) continue;
      if (st == 7) LG_BAR;
      if (st == 9) STAMP(9);
#pragma unroll
      for (int m = 0; m < 2; m++)
#pragma unroll
        for (int c = 0; c < 4; c++) {
          acc[c][m] = MFMA(af[st][c], __builtin_bit_cast(bf16x8, hf[st][m]), acc[c][m]);
          SCHED;
          if (st >= 3 && st + 1 < 14 && m == 0) { read_w(st + 1, c); SCHED; }
        }
      if (st + DEP < 14 && st >= OWN) load_h(st + DEP);
    }
    SCHED;
    STAMP(10);
    const unsigned ro = ((0x3052 >> (4 * q)) & 15) * UNIT + (32 * mb + (l & 15)) * 256 + ((g ^ (l & 7)) * 16);
#pragma unroll
    for (int m = 0; m < 2; m++)
#pragma unroll
      for (int c = 0; c < 4; c++) *(LAS f32x4*)(sm + (ro ^ ((c & 1) * 64)) + m * 4096 + (c >> 1) * 128) = acc[c][m];
    LG_BAR;
    const unsigned rr = (8 * w + (l >> 3)) * 256 + (((2 * (l & 7)) ^ (l >> 3)) * 16);
    f32x4 s0 = *(LAS f32x4*)(sm + rr), s1 = *(LAS f32x4*)(sm + (rr ^ 16));
#pragma unroll
    for (int k = 1; k < 4; k++) { const unsigned u = (k == 1 ? 2 : k == 2 ? 3 : 5) * UNIT; s0 += *(LAS f32x4*)(sm + rr + u); s1 += *(LAS f32x4*)(sm + (rr ^ 16) + u); }
    const u32x4 o = {pk2((f32x2){s0[0], s0[1]}), pk2((f32x2){s0[2], s0[3]}), pk2((f32x2){s1[0], s1[1]}), pk2((f32x2){s1[2], s1[3]})};
    __builtin_amdgcn_raw_buffer_store_b128(o, __builtin_amdgcn_make_buffer_rsrc(out, 0, 2048 * 1024, 0x00020000), (r2 * 64 + 8 * w + (l >> 3)) * 1024 + c2 * 128 + (l & 7) * 16, 0, OAUX);
  }
}

extern "C" void kernel_launch(void* const* d_in, const int* in_sizes, int n_in, void* d_out, int out_size,
                              void* d_ws, size_t ws_size, hipStream_t stream) {
  mlp_fused<<<256, 768, 0, stream>>>((const char*)d_in[0], (const char*)d_in[1], (const char*)d_in[2],
                                     (const char*)d_in[3], (char*)d_out, (char*)d_ws);
}
```

```cpp
#include <hip/hip_runtime.h>
#include <stdint.h>

typedef short    s16x4  __attribute__((ext_vector_type(4)));
typedef __bf16   bf16x8 __attribute__((ext_vector_type(8)));
typedef __bf16   bf16x2 __attribute__((ext_vector_type(2)));
typedef float    f32x2  __attribute__((ext_vector_type(2)));
typedef float    f32x4  __attribute__((ext_vector_type(4)));
typedef unsigned u32x4  __attribute__((ext_vector_type(4)));
typedef unsigned u32x2  __attribute__((ext_vector_type(2)));
typedef unsigned u32x8  __attribute__((ext_vector_type(8)));
typedef unsigned long long u64;
#define LAS __attribute__((address_space(3)))
#define GAS __attribute__((address_space(1)))
#define CAS __attribute__((address_space(4)))
#define DI __device__ __forceinline__

constexpr unsigned UNIT = 16384;
constexpr unsigned HP = 2048;
constexpr unsigned H_BYTES = 32u * 14u * 16u * HP;
constexpr unsigned FLAG_OFF = 16u << 20;
constexpr int CODE_SECT = 197;
constexpr int PRE = 8;
constexpr int DEP = 4;
constexpr int SC1 = 16;

DI void glds4(unsigned ldsaddr, const char* base, const unsigned (&v)[4]) {
  asm volatile("s_mov_b32 m0, %0\n\ts_nop 0\n\t"
               "global_load_lds_dwordx4 %2, %1\n\t"
               "global_load_lds_dwordx4 %3, %1 offset:1024\n\t"
               "global_load_lds_dwordx4 %4, %1 offset:2048\n\t"
               "global_load_lds_dwordx4 %5, %1 offset:3072"
               :: "s"(__builtin_amdgcn_readfirstlane(ldsaddr)), "s"(base), "v"(v[0]), "v"(v[1]), "v"(v[2]), "v"(v[3]) : "memory");
}
DI void glds1(const char* base, unsigned v) { asm volatile("global_load_lds_dwordx4 %0, %1" :: "v"(v), "s"(base) : "memory"); }
#define VM_BAR(n) asm volatile("s_waitcnt vmcnt(%0)\n\ts_barrier" :: "i"(n) : "memory")
#define VM_WAIT(n) asm volatile("s_waitcnt vmcnt(%0)" :: "i"(n) : "memory")
#define LG_BAR do { __builtin_amdgcn_s_waitcnt(0xC07F); asm volatile("s_barrier" ::: "memory"); } while (0)
#define BAR asm volatile("s_barrier" ::: "memory")
#define SCHED __builtin_amdgcn_sched_barrier(0)
#define MFMA(a, b, c) __builtin_amdgcn_mfma_f32_16x16x32_bf16(a, b, c, 0, 0, 0)

DI bf16x8 tr_frag(LAS char* p0, LAS char* p1) {
  s16x4 a = __builtin_amdgcn_ds_read_tr16_b64_v4i16((LAS s16x4*)p0);
  s16x4 b = __builtin_amdgcn_ds_read_tr16_b64_v4i16((LAS s16x4*)p1);
  return __builtin_bit_cast(bf16x8, __builtin_shufflevector(a, b, 0, 1, 2, 3, 4, 5, 6, 7));
}
DI unsigned pk2(f32x2 v) { return __builtin_bit_cast(unsigned, __builtin_convertvector(v, bf16x2)); }
DI unsigned mish2(float h0, float h1) {
  const f32x2 h = {h0, h1};
  const f32x2 t = h * 1.44269504089f + (-14.f);
  const f32x2 E = {__builtin_amdgcn_fmed3f(__builtin_amdgcn_exp2f(t.x), 0.f, 1.f), __builtin_amdgcn_fmed3f(__builtin_amdgcn_exp2f(t.y), 0.f, 1.f)};
  const f32x2 W = E + 0x1p-13f;
  const f32x2 D = E * W + 0x1p-27f;
  const f32x2 R = {__builtin_amdgcn_rcpf(D.x), __builtin_amdgcn_rcpf(D.y)};
  return pk2(h * (E * (W * R)));
}
template <int N> struct IC { static constexpr int v = N; };
template <int A, int B, class F> DI void sfor(F&& f) { if constexpr (A < B) { f(IC<A>{}); sfor<A + 1, B>(f); } }
constexpr int act_first(int cycles) {
  int n = 0;
  for (int c = 0; n < 2 * 34 && c < cycles; n++) c += (n % 34) >> 2 == 1 || (n % 34) >> 2 == 4 ? 8 : 4;
  return n;
}
DI const char* ptr(unsigned lo, unsigned hi) { return (const char*)(((u64)hi << 32) | lo); }

DI constexpr int w2unit(int st) { constexpr int U[14] = {6, 7, 8, 9, 2, 5, 0, 3, 1, 4, 6, 7, 8, 9}; return U[st]; }

__global__ __launch_bounds__(768, 3) void mlp_fused(const char* __restrict__, const char* __restrict__, const char* __restrict__,
                                                    const char* __restrict__, char* __restrict__, char*) {
  __shared__ __attribute__((aligned(1024))) char smem[163840];
  LAS char* sm = (LAS char*)smem;
  const unsigned lds0 = (unsigned)(size_t)sm;
  asm volatile("v_readfirstlane_b32 s95, %1\n\ts_cmp_lg_u32 s95, 0\n\ts_cbranch_scc1 1f\n\ts_load_dword s95, %0, 0\n1:"
               :: "s"(__builtin_amdgcn_kernarg_segment_ptr()), "v"(threadIdx.x) : "s95", "scc");
#define ARGR(id) u32x8 ka; u32x4 kb; \
  asm volatile("s_load_dwordx8 %0, %2, 0\n\ts_load_dwordx4 %1, %2, 0x20 ; " id : "=&s"(ka), "=&s"(kb) : "s"(__builtin_amdgcn_kernarg_segment_ptr())); SCHED
#define ARGS asm volatile("s_waitcnt lgkmcnt(0)" : "+s"(ka), "+s"(kb)); \
  const char* x = ptr(ka[0], ka[1]); const char* w1 = ptr(ka[2], ka[3]); const char* b1 = ptr(ka[4], ka[5]); const char* w2 = ptr(ka[6], ka[7]); \
  char* out = (char*)ptr(kb[0], kb[1]); char* ws = (char*)ptr(kb[2], kb[3]); (void)x, (void)w1, (void)b1, (void)w2, (void)out
#define GEOM(id) int b = blockIdx.x, l = threadIdx.x; asm volatile("; " id : "+s"(b)); asm volatile("; " id : "+v"(l)); l &= 63; \
  const int xcd = b & 7, i = b >> 3, rt = (xcd >> 1) * 4 + (i & 3), ct = (-(xcd & 1) & 7) + (i >> 2), r2 = xcd * 4 + (i >> 3), c2 = i & 7
  const int w = __builtin_amdgcn_readfirstlane(threadIdx.x >> 6);

  if (__builtin_expect(w >= 8, 1)) {
    ARGR("loader");
    GEOM("loader");
    (void)r2;
    const int j = w - 8;
    const unsigned ldsw = lds0 + j * 4096;
    unsigned vw2[4], vx[4], vw[4];
    vx[0] = (32 * j + (l >> 3)) * 1024 + (((l & 7) ^ (l >> 4)) * 16);
    vw[0] = (16 * j + (l >> 4)) * 3584 + (((l & 15) ^ ((l >> 4) << 2)) * 16);
#pragma unroll
    for (int k = 1; k < 4; k++) { vx[k] = (vx[0] ^ ((k & 1) * 64)) + k * 7168; vw[k] = (vw[0] ^ (k * 16)) + k * (4 * 3584 - 1024); }
    int rot = 4 * ((i + (i >> 2)) & 1) + (((xcd >> 1) + 2 * (xcd & 1)) & 3);
    unsigned x0 = rt * 131072 + rot * 128, w0 = ct * 256 + rot * (64 * 3584);
    asm volatile("" : "+v"(vx[0]), "+v"(vx[1]), "+v"(vx[2]), "+v"(vx[3]), "+v"(vw[0]), "+v"(vw[1]), "+v"(vw[2]), "+v"(vw[3]));
    asm volatile("" : "+s"(x0), "+s"(w0), "+s"(rot));
    ARGS;
    const char* w2c = w2 + c2 * 128;
    auto stage_w2 = [&](int st) { glds4(ldsw + w2unit(st) * UNIT, w2c + st * 131072, vw2); };
    auto init_w2 = [&]() {
#pragma unroll
      for (int k = 0; k < 4; k++) vw2[k] = (32 * j + (l >> 3)) * 1024 + (((l & 7) ^ (((l >> 4) & 3) << 1)) * 16) + k * 7168;
    };
    if (__builtin_expect(i < 28, 1)) {
      const char* xb = x + x0;
      const char* wb = w1 + w0;
      auto stage1 = [&](int s) {
        const int k = s == 0 || s + rot < 8 ? s : s - 8;
        glds4(ldsw + (s % 3) * UNIT, xb + k * 128, vx);
        glds4(ldsw + (3 + s % 3) * UNIT, wb + k * (64 * 3584), vw);
      };
      stage1(0);
      if (j == 1) { u64 pc; asm volatile("s_getpc_b64 %0" : "=s"(pc));
        asm volatile("s_mov_b32 m0, %2\n\ts_mov_b64 exec, 0xff\n\tglobal_load_lds_dword %0, %1\n\ts_mov_b64 exec, -1"
                     :: "v"((unsigned)min(i * 7 + l, CODE_SECT - 1) * 64), "s"(pc), "s"(lds0 + 9 * UNIT + 4096) : "memory"); }
      BAR;
      stage1(1);
      init_w2();
#pragma unroll
      for (int s = 0; s < 8; s++) {
        VM_BAR(8);
        if (s < 6) stage1(s + 2);
        if (s == 6) { stage_w2(0); stage_w2(1); }
      }
#pragma unroll
      for (int st = 2; st < PRE; st++) stage_w2(st);
      VM_BAR(4 * (PRE - 4));
      BAR;
#pragma unroll
      for (int st = PRE; st < 14; st++) stage_w2(st);
    } else {
      init_w2();
      BAR;
#pragma nounroll
      for (int st = 0; st < 14; st++) {
        if (st == 4) VM_BAR(0);
        if (st == 10) BAR;
        const int u = (int)(0x98764130529876ull >> (4 * st)) & 15;
        glds4(ldsw + u * UNIT, w2c + st * 131072, vw2);
        VM_WAIT(8);
      }
    }
    VM_BAR(24);
    VM_BAR(0);
    if (b == 255 && j == 0) {
      GAS u64* flags = (GAS u64*)(ws + FLAG_OFF);
      const u64 tag = __hip_atomic_load(flags + 256 * 16, __ATOMIC_RELAXED, __HIP_MEMORY_SCOPE_AGENT) + 1;
#pragma nounroll
      for (int spins = 0; spins < (1 << 18); spins++) {
        u64 v[4];
#pragma unroll
        for (int k = 0; k < 4; k++) v[k] = __hip_atomic_load(flags + (k * 64 + l) * 16, __ATOMIC_RELAXED, __HIP_MEMORY_SCOPE_AGENT);
        if (__all(v[0] == tag && v[1] == tag && v[2] == tag && v[3] == tag)) break;
      }
      if (l == 0) __hip_atomic_store(flags + 256 * 16, tag, __ATOMIC_RELAXED, __HIP_MEMORY_SCOPE_AGENT);
    }
    return;
  }

  ARGR("compute");
  GEOM("compute");
  const int g = l >> 4, q4 = (l & 15) >> 2, p = l & 3;
  const unsigned hl = l * 16 + (l >= 24 ? 128 : 0);
  ARGS;
  GAS u64* flags = (GAS u64*)(ws + FLAG_OFF);
  const __amdgpu_buffer_rsrc_t hrs = __builtin_amdgcn_make_buffer_rsrc(ws, 0, H_BYTES, 0x00020000);
  u64 tag = *(const CAS u64*)(size_t)(ws + FLAG_OFF + 256 * 128);

  if (__builtin_expect(i < 28, 1)) {
    const int fq = w & 3, mh = w >> 2;
    u32x2 bb[2];
#pragma unroll
    for (int fb = 0; fb < 2; fb++) bb[fb] = *(const GAS u32x2*)(b1 + (ct * 128 + fq * 32 + fb * 16 + g * 4) * 2);
    SCHED;
    LAS char* xa[2];
#pragma unroll
    for (int ks = 0; ks < 2; ks++) xa[ks] = sm + (64 * mh + (l & 15)) * 128 + (((4 * ks + g) ^ ((l >> 1) & 7)) * 16);
    LAS char* wa[2][2];
#pragma unroll
    for (int fb = 0; fb < 2; fb++)
#pragma unroll
      for (int t = 0; t < 2; t++) {
        unsigned o = 3 * UNIT + (8 * g + 4 * t + q4) * 256 + (((4 * fq + 2 * fb + (p >> 1)) ^ ((q4 << 2) | ((2 * g + t) & 3))) * 16) + 8 * (p & 1);
        asm("" : "+v"(o));
        wa[fb][t] = sm + o;
      }
    const unsigned hv = hl + (((rt * 2 + mh) * 14 + ct) * 4 + fq) * 4 * HP;
    SCHED;
    BAR;
    asm volatile("" : "+s"(tag));
    tag += 1;
    f32x4 acc[2][4];
    bf16x8 a[2][2][2], bx[2][2][4];
    sfor<0, 8>([&](auto S) {
      constexpr int s = S.v, c = s & 1;
      SCHED;
      LG_BAR;
      constexpr unsigned so = (s % 3) * UNIT;
      auto rda = [&](int ks, int fb) { a[c][ks][fb] = tr_frag(wa[fb][0] + so + ks * 8192, wa[fb][1] + so + ks * 8192); SCHED; };
      auto rdb = [&](int ks, int m) { bx[c][ks][m] = *(LAS bf16x8*)(xa[ks] + so + m * 2048); SCHED; };
      auto late = [&](int n) { const int m = n >> 1, fb = n & 1; if (s > 0) { acc[fb][m] = MFMA(a[c ^ 1][1][fb], bx[c ^ 1][1][m], acc[fb][m]); SCHED; } };
      auto mm = [&](int n) { const int m = n >> 1, fb = n & 1; acc[fb][m] = MFMA(a[c][0][fb], bx[c][0][m], acc[fb][m]); SCHED; };
      if constexpr (s < 7) {
        late(0); rda(0, 0); rdb(0, 0); late(1); rda(0, 1); late(2); rdb(0, 1); late(3); rdb(0, 2); late(4); rdb(0, 3); late(5); rda(1, 0); late(6); rdb(1, 0); late(7); rda(1, 1);
        if (s == 0) {
#pragma unroll
          for (int fb = 0; fb < 2; fb++)
#pragma unroll
            for (int m = 0; m < 4; m++)
              acc[fb][m] = (f32x4){__uint_as_float(bb[fb][0] << 16), __uint_as_float(bb[fb][0] & 0xffff0000u), __uint_as_float(bb[fb][1] << 16), __uint_as_float(bb[fb][1] & 0xffff0000u)};
          SCHED;
        }
        mm(0); rdb(1, 1); mm(1); rdb(1, 2); mm(2); rdb(1, 3); mm(3); mm(4); mm(5); mm(6); mm(7);
      } else {
        late(0); rda(0, 0); rdb(0, 0); late(1); rda(0, 1); late(2); rda(1, 0); late(3); rdb(1, 0); late(4); rda(1, 1); late(5); rdb(0, 1); late(6); rdb(1, 1); late(7);
        rdb(0, 2); rdb(1, 2); rdb(0, 3); rdb(1, 3);
      }
    });
    auto last = [&](int m, int n) { acc[n & 1][m] = MFMA(a[1][n >> 1][n & 1], bx[1][n >> 1][m], acc[n & 1][m]); };
    last(0, 0); last(0, 1); last(0, 2); last(0, 3);
    SCHED;
    u32x4 o;
    float tE[4], tW[4], tD[4], tU[4];
    auto piece = [&](auto N) {
      constexpr int n = N.v, fb = n / 34, r = n % 34, op = r >> 2, e = r & 3;
      const float h = acc[fb][0][e];
      if constexpr (op == 0) tE[e] = __builtin_fmaf(h, 1.44269504089f, -14.f);
      if constexpr (op == 1) tE[e] = __builtin_amdgcn_fmed3f(__builtin_amdgcn_exp2f(tE[e]), 0.f, 1.f);
      if constexpr (op == 2) tW[e] = tE[e] + 0x1p-13f;
      if constexpr (op == 3) tD[e] = __builtin_fmaf(tE[e], tW[e], 0x1p-27f);
      if constexpr (op == 4) tD[e] = __builtin_amdgcn_rcpf(tD[e]);
      if constexpr (op == 5) tW[e] *= tD[e];
      if constexpr (op == 6) tE[e] *= tW[e];
      if constexpr (op == 7) { tU[e] = h * tE[e]; asm("" : "+v"(tU[e])); }
      if constexpr (op == 8) o[fb * 2 + e] = pk2((f32x2){tU[2 * e], tU[2 * e + 1]});
    };
    sfor<0, 12>([&](auto K) {
      constexpr int k = K.v;
      last(1 + (k >> 2), k & 3);
      SCHED;
      sfor<act_first(12 * k), act_first(k == 11 ? 1000 : 12 * k + 12)>(piece);
      SCHED;
    });
#pragma unroll
    for (int m = 0; m < 4; m++) {
      if (m > 0) o = (u32x4){mish2(acc[0][m][0], acc[0][m][1]), mish2(acc[0][m][2], acc[0][m][3]), mish2(acc[1][m][0], acc[1][m][1]), mish2(acc[1][m][2], acc[1][m][3])};
      __builtin_amdgcn_raw_buffer_store_b128(o, hrs, hv + m * HP, 0, SC1);
      SCHED;
    }
  } else {
    const unsigned hh = i - 28, cs = (3 - (unsigned)((size_t)w2 >> 7)) & 7;
    const unsigned v2 = ((w >> 2) * 128 + xcd * 16 + hh * 4 + (w & 3)) * 1024 + l * 16;
    const unsigned v3 = ((w >> 2) * 128 + hh * 32 + (w & 3) * 8 + (l >> 3)) * 1024 + cs * 128 + (l & 7) * 16;
    asm volatile("s_mov_b32 m0, %0" :: "s"(lds0 + w * 1024) : "memory");
    __builtin_amdgcn_s_sleep(10);
#pragma unroll
    for (int h = 0; h < 2; h++) {
#pragma unroll
      for (int k = 4 * h; k < 4 + 3 * h; k++) glds1(w2 + k * 262144, v2);
#pragma unroll
      for (int k = 4 * h; k < 4 + 3 * h; k++) glds1(w2 + k * 262144, v3);
    }
    BAR;
    asm volatile("" : "+s"(tag));
    tag += 1;
    VM_WAIT(0);
  }
  SCHED;
  BAR;
  const int q = w & 3, mb = w >> 2;
  LAS char* wb[3][4];
#pragma unroll
  for (int k = 0; k < 3; k++)
#pragma unroll
    for (int c = 0; c < 4; c++) {
      unsigned o = k * 65536 + (32 * q + 4 * g + q4) * 128 + (((2 * c + (p >> 1)) ^ ((((2 * g) | (q4 >> 1)) & 3) << 1)) * 16) + 8 * (p & 1);
      asm("" : "+v"(o));
      wb[k][c] = sm + o;
    }
  bf16x8 af[14][4];
  auto read_w = [&](int st, int c) {
    const int u = w2unit(st);
    LAS char* a = wb[u >> 2][c] + (u & 3) * UNIT;
    af[st][c] = tr_frag(a, a + 2048);
  };
#pragma unroll
  for (int st = 0; st < 4; st++)
#pragma unroll
    for (int c = 0; c < 4; c++) read_w(st, c);
  SCHED;
  const int hid = xcd * 4 + (i - 28), slot = i < 28 ? rt * 16 + ct : (hid >> 1) * 16 + 14 + (hid & 1);
  asm volatile("s_cmp_eq_u32 %0, 0\n\ts_waitcnt vmcnt(0) lgkmcnt(0)\n\ts_barrier\n\ts_cbranch_scc0 1f\n\t"
               "s_mov_b64 exec, 1\n\tglobal_store_dwordx2 %1, %2, %3 sc1\n\ts_mov_b64 exec, -1\n1:"
               :: "s"(w), "v"(slot * 128), "v"(tag), "s"(flags) : "memory", "scc");
  if (w == 1) {
    GAS u64* fp = flags + ((r2 >> 1) * 16 + (l < 14 ? l : 0)) * 16;
    if (i >= 28) __builtin_amdgcn_s_sleep(20);
    __builtin_amdgcn_s_sleep(20);
#pragma nounroll
    for (int spins = 0; spins < (1 << 18); spins++)
      if (__all(__hip_atomic_load(fp, __ATOMIC_RELAXED, __HIP_MEMORY_SCOPE_AGENT) == tag)) break;
  }
  SCHED;
  BAR;
  const unsigned hv = hl + (q * 2 + mb) * 2 * HP, hs = r2 * 14 * 16 * HP;
  u32x4 hf[14][2];
  auto load_h = [&](int st) {
    hf[st][0] = __builtin_amdgcn_raw_buffer_load_b128(hrs, hv, hs + st * 16 * HP, SC1);
    hf[st][1] = __builtin_amdgcn_raw_buffer_load_b128(hrs, hv + HP, hs + st * 16 * HP, SC1);
  };
#pragma unroll
  for (int st = 0; st < DEP; st++) load_h(st);
  f32x4 acc[4][2] = {};
  unsigned ro[2], rr[2][2], ov;
  auto put = [&](int c, int m) { *(LAS f32x4*)(sm + ro[c & 1] + m * 4096 + (c >> 1) * 128) = acc[c][m]; };
#pragma unroll
  for (int st = 0; st < 14; st++) {
    SCHED;
    if (st == 7) LG_BAR;
#pragma unroll
    for (int m = 0; m < 2; m++)
#pragma unroll
      for (int c = 0; c < 4; c++) {
        acc[c][m] = MFMA(af[st][c], __builtin_bit_cast(bf16x8, hf[st][m]), acc[c][m]);
        SCHED;
        if (st >= 3 && st < 13 && m == 0) { read_w(st + 1, c); SCHED; }
        if (st == 13 && m == 1) { put(c, 0); SCHED; }
      }
    if (st + DEP < 14) load_h(st + DEP);
    if (st == 10) {
      ro[0] = ((0x3052 >> (4 * q)) & 15) * UNIT + (32 * mb + (l & 15)) * 256 + ((g ^ (l & 7)) * 16);
      ro[1] = ro[0] ^ 64;
      rr[0][0] = (8 * w + (l >> 3)) * 256 + (((2 * (l & 7)) ^ (l >> 3)) * 16);
      rr[0][1] = rr[0][0] ^ 16; rr[1][0] = rr[0][0] + 5 * UNIT; rr[1][1] = rr[0][1] + 5 * UNIT;
      ov = (r2 * 64 + 8 * w + (l >> 3)) * 1024 + c2 * 128 + (l & 7) * 16;
      asm volatile("" : "+v"(ro[0]), "+v"(ro[1]), "+v"(rr[0][0]), "+v"(rr[0][1]), "+v"(rr[1][0]), "+v"(rr[1][1]), "+v"(ov));
    }
  }
  SCHED;
#pragma unroll
  for (int c = 0; c < 4; c++) put(c, 1);
  SCHED;
  LG_BAR;
  f32x4 s[4][2];
#pragma unroll
  for (int k = 0; k < 4; k++)
#pragma unroll
    for (int h = 0; h < 2; h++) s[k][h] = *(LAS f32x4*)(sm + rr[k == 3][h] + (k == 1 ? 2 : k == 2 ? 3 : 0) * UNIT);
  SCHED;
  const f32x4 s0 = (s[0][0] + s[1][0]) + (s[2][0] + s[3][0]), s1 = (s[0][1] + s[1][1]) + (s[2][1] + s[3][1]);
  const u32x4 o = {pk2((f32x2){s0[0], s0[1]}), pk2((f32x2){s0[2], s0[3]}), pk2((f32x2){s1[0], s1[1]}), pk2((f32x2){s1[2], s1[3]})};
  __builtin_amdgcn_raw_buffer_store_b128(o, __builtin_amdgcn_make_buffer_rsrc(out, 0, 2048 * 1024, 0x00020000), ov, 0, SC1);
}

extern "C" void kernel_launch(void* const* d_in, const int* in_sizes, int n_in, void* d_out, int out_size,
                              void* d_ws, size_t ws_size, hipStream_t stream) {
  mlp_fused<<<256, 768, 0, stream>>>((const char*)d_in[0], (const char*)d_in[1], (const char*)d_in[2],
                                     (const char*)d_in[3], (char*)d_out, (char*)d_ws);
}
```

```cpp
#include <hip/hip_runtime.h>
#include <hip/hip_bf16.h>
typedef unsigned short u16; typedef unsigned long long u64;
#define BLOCK 512
#define LDSB 163840
#define GRID 256
#define XT 100000
__device__ __forceinline__ float bf2f(u16 v){ return __uint_as_float(((unsigned)v)<<16); }
__device__ __forceinline__ u16 f2bf(float f){ unsigned u=__float_as_uint(f); u += 0x7fff + ((u>>16)&1); return u>>16; }
__global__ __launch_bounds__(BLOCK) void k(const u16* __restrict__ x, const u16* __restrict__ w1, const u16* __restrict__ b1, const u16* __restrict__ w2, u16* __restrict__ out){
  u64 t0; asm volatile("s_memrealtime %0\n\ts_waitcnt lgkmcnt(0)" : "=s"(t0) :: "memory");
  constexpr int R = 2048 / GRID;
  __shared__ char smem[LDSB];
  u16* hs = (u16*)smem;
  const int r0 = blockIdx.x * R;
  for (int f = threadIdx.x; f < 1792; f += BLOCK) {
    float a[R]; for (int r = 0; r < R; r++) a[r] = bf2f(b1[f]);
    for (int kk = 0; kk < 512; kk++) { float w = bf2f(w1[kk * 1792 + f]); for (int r = 0; r < R; r++) a[r] += bf2f(x[(r0 + r) * 512 + kk]) * w; }
    for (int r = 0; r < R; r++) { float h = a[r]; float e = __expf(fminf(h, 30.f)); float n = e * (e + 2.f); hs[r * 1792 + f] = f2bf(h * n / (n + 2.f)); }
  }
  __syncthreads();
  for (int n = threadIdx.x; n < 512; n += BLOCK) {
    float a[R]; for (int r = 0; r < R; r++) a[r] = 0;
    for (int kk = 0; kk < 1792; kk++) { float w = bf2f(w2[kk * 512 + n]); for (int r = 0; r < R; r++) a[r] += bf2f(hs[r * 1792 + kk]) * w; }
    for (int r = 0; r < R; r++) __hip_atomic_store(out + (r0 + r) * 512 + n, f2bf(a[r]), __ATOMIC_RELAXED, __HIP_MEMORY_SCOPE_AGENT);
  }
  asm volatile("s_waitcnt vmcnt(0)" ::: "memory");
  u64 t1; do { __builtin_amdgcn_s_sleep(1); asm volatile("s_memrealtime %0\n\ts_waitcnt lgkmcnt(0)" : "=s"(t1) :: "memory"); } while (t1 < t0 + XT);
}
extern "C" void kernel_launch(void* const* d_in, const int* in_sizes, int n_in, void* d_out, int out_size, void* d_ws, size_t ws_size, hipStream_t stream) {
  k<<<GRID, BLOCK, 0, stream>>>((const u16*)d_in[0], (const u16*)d_in[1], (const u16*)d_in[2], (const u16*)d_in[3], (u16*)d_out);
}
```

```cpp
#include <hip/hip_runtime.h>
#include <stdint.h>

typedef short    s16x4  __attribute__((ext_vector_type(4)));
typedef __bf16   bf16x8 __attribute__((ext_vector_type(8)));
typedef __bf16   bf16x2 __attribute__((ext_vector_type(2)));
typedef float    f32x2  __attribute__((ext_vector_type(2)));
typedef float    f32x4  __attribute__((ext_vector_type(4)));
typedef unsigned u32x4  __attribute__((ext_vector_type(4)));
typedef unsigned u32x2  __attribute__((ext_vector_type(2)));
typedef unsigned u32x8  __attribute__((ext_vector_type(8)));
typedef unsigned long long u64;
#define LAS __attribute__((address_space(3)))
#define GAS __attribute__((address_space(1)))
#define CAS __attribute__((address_space(4)))
#define DI __device__ __forceinline__

constexpr unsigned UNIT = 16384;
constexpr unsigned HP = 2048;
constexpr unsigned H_BYTES = 32u * 14u * 16u * HP;
constexpr unsigned FLAG_OFF = 16u << 20;
constexpr int CODE_SECT = 165;
constexpr int PRE = 8;
constexpr int DEP = 4;
constexpr int SC1 = 16;

DI void glds4(unsigned ldsaddr, const char* base, const unsigned (&v)[4]) {
  asm volatile("s_mov_b32 m0, %0\n\ts_nop 0\n\t"
               "global_load_lds_dwordx4 %2, %1\n\t"
               "global_load_lds_dwordx4 %3, %1 offset:1024\n\t"
               "global_load_lds_dwordx4 %4, %1 offset:2048\n\t"
               "global_load_lds_dwordx4 %5, %1 offset:3072"
               :: "s"(__builtin_amdgcn_readfirstlane(ldsaddr)), "s"(base), "v"(v[0]), "v"(v[1]), "v"(v[2]), "v"(v[3]) : "memory");
}
DI void glds1(const char* base, unsigned v) { asm volatile("global_load_lds_dwordx4 %0, %1" :: "v"(v), "s"(base) : "memory"); }
#define VM_BAR(n) asm volatile("s_waitcnt vmcnt(%0)\n\ts_barrier" :: "i"(n) : "memory")
#define VM_WAIT(n) asm volatile("s_waitcnt vmcnt(%0)" :: "i"(n) : "memory")
#define LG_BAR do { __builtin_amdgcn_s_waitcnt(0xC07F); asm volatile("s_barrier" ::: "memory"); } while (0)
#define BAR asm volatile("s_barrier" ::: "memory")
#define SCHED __builtin_amdgcn_sched_barrier(0)
#define MFMA(a, b, c) __builtin_amdgcn_mfma_f32_16x16x32_bf16(a, b, c, 0, 0, 0)

DI bf16x8 tr_frag(LAS char* p0, LAS char* p1) {
  s16x4 a = __builtin_amdgcn_ds_read_tr16_b64_v4i16((LAS s16x4*)p0);
  s16x4 b = __builtin_amdgcn_ds_read_tr16_b64_v4i16((LAS s16x4*)p1);
  return __builtin_bit_cast(bf16x8, __builtin_shufflevector(a, b, 0, 1, 2, 3, 4, 5, 6, 7));
}
DI unsigned pk2(f32x2 v) { return __builtin_bit_cast(unsigned, __builtin_convertvector(v, bf16x2)); }
DI unsigned mish2(float h0, float h1) {
  const f32x2 h = {h0, h1};
  const f32x2 t = h * 1.44269504089f + (-14.f);
  const f32x2 E = {__builtin_amdgcn_fmed3f(__builtin_amdgcn_exp2f(t.x), 0.f, 1.f), __builtin_amdgcn_fmed3f(__builtin_amdgcn_exp2f(t.y), 0.f, 1.f)};
  const f32x2 W = E + 0x1p-13f;
  const f32x2 D = E * W + 0x1p-27f;
  const f32x2 R = {__builtin_amdgcn_rcpf(D.x), __builtin_amdgcn_rcpf(D.y)};
  return pk2(h * (E * (W * R)));
}
template <int N> struct IC { static constexpr int v = N; };
template <int A, int B, class F> DI void sfor(F&& f) { if constexpr (A < B) { f(IC<A>{}); sfor<A + 1, B>(f); } }
constexpr int act_first(int cycles) {
  int n = 0;
  for (int c = 0; n < 2 * 34 && c < cycles; n++) c += (n % 34) >> 2 == 1 || (n % 34) >> 2 == 4 ? 8 : 4;
  return n;
}
DI const char* ptr(unsigned lo, unsigned hi) { return (const char*)(((u64)hi << 32) | lo); }

DI constexpr int w2unit(int st) { constexpr int U[14] = {6, 7, 8, 9, 2, 5, 0, 3, 1, 4, 6, 7, 8, 9}; return U[st]; }

__global__ __launch_bounds__(768, 3) void mlp_fused(const char* __restrict__, const char* __restrict__, const char* __restrict__,
                                                    const char* __restrict__, char* __restrict__, char*) {
  __shared__ __attribute__((aligned(1024))) char smem[163840];
  LAS char* sm = (LAS char*)smem;
  const unsigned lds0 = (unsigned)(size_t)sm;
  asm volatile("v_readfirstlane_b32 s95, %1\n\ts_cmp_lg_u32 s95, 0\n\ts_cbranch_scc1 1f\n\ts_load_dword s95, %0, 0\n1:"
               :: "s"(__builtin_amdgcn_kernarg_segment_ptr()), "v"(threadIdx.x) : "s95", "scc");
#define ARGR(id) u32x8 ka; u32x4 kb; \
  asm volatile("s_load_dwordx8 %0, %2, 0\n\ts_load_dwordx4 %1, %2, 0x20 ; " id : "=&s"(ka), "=&s"(kb) : "s"(__builtin_amdgcn_kernarg_segment_ptr())); SCHED
#define ARGS asm volatile("s_waitcnt lgkmcnt(0)" : "+s"(ka), "+s"(kb)); \
  const char* x = ptr(ka[0], ka[1]); const char* w1 = ptr(ka[2], ka[3]); const char* b1 = ptr(ka[4], ka[5]); const char* w2 = ptr(ka[6], ka[7]); \
  char* out = (char*)ptr(kb[0], kb[1]); char* ws = (char*)ptr(kb[2], kb[3]); (void)x, (void)w1, (void)b1, (void)w2, (void)out
#define GEOM(id) int b = blockIdx.x, l = threadIdx.x; asm volatile("; " id : "+s"(b)); asm volatile("; " id : "+v"(l)); l &= 63; \
  const int xcd = b & 7, i = b >> 3, rt = (xcd >> 1) * 4 + (i & 3), ct = (-(xcd & 1) & 7) + (i >> 2), r2 = xcd * 4 + (i >> 3), c2 = i & 7
  const int w = __builtin_amdgcn_readfirstlane(threadIdx.x >> 6);

  if (__builtin_expect(w >= 8, 1)) {
    ARGR("loader");
    GEOM("loader");
    (void)r2;
    const int j = w - 8;
    const unsigned ldsw = lds0 + j * 4096;
    unsigned vw2[4], vx[4], vw[4];
    vx[0] = (32 * j + (l >> 3)) * 1024 + (((l & 7) ^ (l >> 4)) * 16);
    vw[0] = (16 * j + (l >> 4)) * 3584 + (((l & 15) ^ ((l >> 4) << 2)) * 16);
#pragma unroll
    for (int k = 1; k < 4; k++) { vx[k] = (vx[0] ^ ((k & 1) * 64)) + k * 7168; vw[k] = (vw[0] ^ (k * 16)) + k * (4 * 3584 - 1024); }
    int rot = 4 * ((i + (i >> 2)) & 1) + (((xcd >> 1) + 2 * (xcd & 1)) & 3);
    unsigned x0 = rt * 131072 + rot * 128, w0 = ct * 256 + rot * (64 * 3584);
    asm volatile("" : "+v"(vx[0]), "+v"(vx[1]), "+v"(vx[2]), "+v"(vx[3]), "+v"(vw[0]), "+v"(vw[1]), "+v"(vw[2]), "+v"(vw[3]));
    asm volatile("" : "+s"(x0), "+s"(w0), "+s"(rot));
    ARGS;
    const char* w2c = w2 + c2 * 128;
    auto stage_w2 = [&](int st) { glds4(ldsw + w2unit(st) * UNIT, w2c + st * 131072, vw2); };
    auto init_w2 = [&]() {
#pragma unroll
      for (int k = 0; k < 4; k++) vw2[k] = (32 * j + (l >> 3)) * 1024 + (((l & 7) ^ (((l >> 4) & 3) << 1)) * 16) + k * 7168;
    };
    if (__builtin_expect(i < 28, 1)) {
      const char* xb = x + x0;
      const char* wb = w1 + w0;
      auto stage1 = [&](int s) {
        const int k = s == 0 || s + rot < 8 ? s : s - 8;
        glds4(ldsw + (s % 3) * UNIT, xb + k * 128, vx);
        glds4(ldsw + (3 + s % 3) * UNIT, wb + k * (64 * 3584), vw);
      };
      stage1(0);
      if (j == 1) { u64 pc; asm volatile("s_getpc_b64 %0" : "=s"(pc));
        asm volatile("s_mov_b32 m0, %2\n\ts_mov_b64 exec, 0x3f\n\tglobal_load_lds_dword %0, %1\n\ts_mov_b64 exec, -1"
                     :: "v"((unsigned)min(i * 6 + l, CODE_SECT - 1) * 64), "s"(pc), "s"(lds0 + 9 * UNIT + 4096) : "memory"); }
      BAR;
      stage1(1);
      init_w2();
#pragma unroll
      for (int s = 0; s < 8; s++) {
        VM_BAR(8);
        if (s < 6) stage1(s + 2);
        if (s == 6) { stage_w2(0); stage_w2(1); }
      }
#pragma unroll
      for (int st = 2; st < PRE; st++) stage_w2(st);
      VM_BAR(4 * (PRE - 4));
      BAR;
#pragma unroll
      for (int st = PRE; st < 14; st++) stage_w2(st);
    } else {
      init_w2();
      BAR;
#pragma nounroll
      for (int st = 0; st < 14; st++) {
        if (st == 4) VM_BAR(0);
        if (st == 10) BAR;
        const int u = (int)(0x98764130529876ull >> (4 * st)) & 15;
        glds4(ldsw + u * UNIT, w2c + st * 131072, vw2);
        VM_WAIT(8);
      }
    }
    VM_BAR(24);
    VM_BAR(0);
    if (b == 255 && j == 0) {
      GAS u64* flags = (GAS u64*)(ws + FLAG_OFF);
      const u64 tag = __hip_atomic_load(flags + 256 * 16, __ATOMIC_RELAXED, __HIP_MEMORY_SCOPE_AGENT) + 1;
#pragma nounroll
      for (int spins = 0; spins < (1 << 18); spins++) {
        u64 v[4];
#pragma unroll
        for (int k = 0; k < 4; k++) v[k] = __hip_atomic_load(flags + (k * 64 + l) * 16, __ATOMIC_RELAXED, __HIP_MEMORY_SCOPE_AGENT);
        if (__all(v[0] == tag && v[1] == tag && v[2] == tag && v[3] == tag)) break;
      }
      if (l == 0) __hip_atomic_store(flags + 256 * 16, tag, __ATOMIC_RELAXED, __HIP_MEMORY_SCOPE_AGENT);
    }
    return;
  }

  ARGR("compute");
  GEOM("compute");
  const int g = l >> 4, q4 = (l & 15) >> 2, p = l & 3;
  const unsigned hl = l * 16 + (l >= 24 ? 128 : 0);
  ARGS;
  GAS u64* flags = (GAS u64*)(ws + FLAG_OFF);
  const __amdgpu_buffer_rsrc_t hrs = __builtin_amdgcn_make_buffer_rsrc(ws, 0, H_BYTES, 0x00020000);
  u64 tag = *(const CAS u64*)(size_t)(ws + FLAG_OFF + 256 * 128);

  if (__builtin_expect(i < 28, 1)) {
    const int fq = w & 3, mh = w >> 2;
    u32x2 bb[2];
#pragma unroll
    for (int fb = 0; fb < 2; fb++) bb[fb] = *(const GAS u32x2*)(b1 + (ct * 128 + fq * 32 + fb * 16 + g * 4) * 2);
    SCHED;
    LAS char* xa[2];
#pragma unroll
    for (int ks = 0; ks < 2; ks++) xa[ks] = sm + (64 * mh + (l & 15)) * 128 + (((4 * ks + g) ^ ((l >> 1) & 7)) * 16);
    LAS char* wa[2][2];
#pragma unroll
    for (int fb = 0; fb < 2; fb++)
#pragma unroll
      for (int t = 0; t < 2; t++) {
        unsigned o = 3 * UNIT + (8 * g + 4 * t + q4) * 256 + (((4 * fq + 2 * fb + (p >> 1)) ^ ((q4 << 2) | ((2 * g + t) & 3))) * 16) + 8 * (p & 1);
        asm("" : "+v"(o));
        wa[fb][t] = sm + o;
      }
    const unsigned hv = hl + (((rt * 2 + mh) * 14 + ct) * 4 + fq) * 4 * HP;
    SCHED;
    BAR;
    asm volatile("" : "+s"(tag));
    tag += 1;
    f32x4 acc[2][4];
    bf16x8 a[2][2][2], bx[2][2][4];
    sfor<0, 8>([&](auto S) {
      constexpr int s = S.v, c = s & 1;
      SCHED;
      LG_BAR;
      constexpr unsigned so = (s % 3) * UNIT;
      auto rda = [&](int ks, int fb) { a[c][ks][fb] = tr_frag(wa[fb][0] + so + ks * 8192, wa[fb][1] + so + ks * 8192); SCHED; };
      auto rdb = [&](int ks, int m) { bx[c][ks][m] = *(LAS bf16x8*)(xa[ks] + so + m * 2048); SCHED; };
      auto late = [&](int n) { const int m = n >> 1, fb = n & 1; if (s > 0) { acc[fb][m] = MFMA(a[c ^ 1][1][fb], bx[c ^ 1][1][m], acc[fb][m]); SCHED; } };
      auto mm = [&](int n) { const int m = n >> 1, fb = n & 1; acc[fb][m] = MFMA(a[c][0][fb], bx[c][0][m], acc[fb][m]); SCHED; };
      if constexpr (s < 7) {
        late(0); rda(0, 0); rdb(0, 0); late(1); rda(0, 1); late(2); rdb(0, 1); late(3); rdb(0, 2); late(4); rdb(0, 3); late(5); rda(1, 0); late(6); rdb(1, 0); late(7); rda(1, 1);
        if (s == 0) {
#pragma unroll
          for (int fb = 0; fb < 2; fb++)
#pragma unroll
            for (int m = 0; m < 4; m++)
              acc[fb][m] = (f32x4){__uint_as_float(bb[fb][0] << 16), __uint_as_float(bb[fb][0] & 0xffff0000u), __uint_as_float(bb[fb][1] << 16), __uint_as_float(bb[fb][1] & 0xffff0000u)};
          SCHED;
        }
        mm(0); rdb(1, 1); mm(1); rdb(1, 2); mm(2); rdb(1, 3); mm(3); mm(4); mm(5); mm(6); mm(7);
      } else {
        late(0); rda(0, 0); rdb(0, 0); late(1); rda(0, 1); late(2); rda(1, 0); late(3); rdb(1, 0); late(4); rda(1, 1); late(5); rdb(0, 1); late(6); rdb(1, 1); late(7);
        rdb(0, 2); rdb(1, 2); rdb(0, 3); rdb(1, 3);
      }
    });
    auto last = [&](int m, int n) { acc[n & 1][m] = MFMA(a[1][n >> 1][n & 1], bx[1][n >> 1][m], acc[n & 1][m]); };
    last(0, 0); last(0, 1); last(0, 2); last(0, 3);
    SCHED;
    u32x4 o;
    float tE[4], tW[4], tD[4], tU[4];
    auto piece = [&](auto N) {
      constexpr int n = N.v, fb = n / 34, r = n % 34, op = r >> 2, e = r & 3;
      const float h = acc[fb][0][e];
      if constexpr (op == 0) tE[e] = __builtin_fmaf(h, 1.44269504089f, -14.f);
      if constexpr (op == 1) tE[e] = __builtin_amdgcn_fmed3f(__builtin_amdgcn_exp2f(tE[e]), 0.f, 1.f);
      if constexpr (op == 2) tW[e] = tE[e] + 0x1p-13f;
      if constexpr (op == 3) tD[e] = __builtin_fmaf(tE[e], tW[e], 0x1p-27f);
      if constexpr (op == 4) tD[e] = __builtin_amdgcn_rcpf(tD[e]);
      if constexpr (op == 5) tW[e] *= tD[e];
      if constexpr (op == 6) tE[e] *= tW[e];
      if constexpr (op == 7) { tU[e] = h * tE[e]; asm("" : "+v"(tU[e])); }
      if constexpr (op == 8) o[fb * 2 + e] = pk2((f32x2){tU[2 * e], tU[2 * e + 1]});
    };
    sfor<0, 12>([&](auto K) {
      constexpr int k = K.v;
      last(1 + (k >> 2), k & 3);
      SCHED;
      sfor<act_first(12 * k), act_first(k == 11 ? 1000 : 12 * k + 12)>(piece);
      SCHED;
    });
#pragma unroll
    for (int m = 0; m < 4; m++) {
      if (m > 0) o = (u32x4){mish2(acc[0][m][0], acc[0][m][1]), mish2(acc[0][m][2], acc[0][m][3]), mish2(acc[1][m][0], acc[1][m][1]), mish2(acc[1][m][2], acc[1][m][3])};
      __builtin_amdgcn_raw_buffer_store_b128(o, hrs, hv + m * HP, 0, SC1);
      SCHED;
    }
  } else {
    const unsigned hh = i - 28, cs = (3 - (unsigned)((size_t)w2 >> 7)) & 7;
    const unsigned v2 = ((w >> 2) * 128 + xcd * 16 + hh * 4 + (w & 3)) * 1024 + l * 16;
    const unsigned v3 = ((w >> 2) * 128 + hh * 32 + (w & 3) * 8 + (l >> 3)) * 1024 + cs * 128 + (l & 7) * 16;
    asm volatile("s_mov_b32 m0, %0" :: "s"(lds0 + w * 1024) : "memory");
    __builtin_amdgcn_s_sleep(10);
#pragma unroll
    for (int h = 0; h < 2; h++) {
#pragma unroll
      for (int k = 4 * h; k < 4 + 3 * h; k++) glds1(w2 + k * 262144, v2);
#pragma unroll
      for (int k = 4 * h; k < 4 + 3 * h; k++) glds1(w2 + k * 262144, v3);
    }
    BAR;
    asm volatile("" : "+s"(tag));
    tag += 1;
    VM_WAIT(0);
  }
  SCHED;
  BAR;
  const int q = w & 3, mb = w >> 2;
  LAS char* wb[3][4];
#pragma unroll
  for (int k = 0; k < 3; k++)
#pragma unroll
    for (int c = 0; c < 4; c++) {
      unsigned o = k * 65536 + (32 * q + 4 * g + q4) * 128 + (((2 * c + (p >> 1)) ^ ((((2 * g) | (q4 >> 1)) & 3) << 1)) * 16) + 8 * (p & 1);
      asm("" : "+v"(o));
      wb[k][c] = sm + o;
    }
  bf16x8 af[14][4];
  auto read_w = [&](int st, int c) {
    const int u = w2unit(st);
    LAS char* a = wb[u >> 2][c] + (u & 3) * UNIT;
    af[st][c] = tr_frag(a, a + 2048);
  };
#pragma unroll
  for (int st = 0; st < 4; st++)
#pragma unroll
    for (int c = 0; c < 4; c++) read_w(st, c);
  SCHED;
  const int hid = xcd * 4 + (i - 28), slot = i < 28 ? rt * 16 + ct : (hid >> 1) * 16 + 14 + (hid & 1);
  asm volatile("s_cmp_eq_u32 %0, 0\n\ts_waitcnt vmcnt(0) lgkmcnt(0)\n\ts_barrier\n\ts_cbranch_scc0 1f\n\t"
               "s_mov_b64 exec, 1\n\tglobal_store_dwordx2 %1, %2, %3 sc1\n\ts_mov_b64 exec, -1\n1:"
               :: "s"(w), "v"(slot * 128), "v"(tag), "s"(flags) : "memory", "scc");
  if (w == 1) {
    GAS u64* fp = flags + ((r2 >> 1) * 16 + (l < 14 ? l : 0)) * 16;
    if (i >= 28) __builtin_amdgcn_s_sleep(20);
    __builtin_amdgcn_s_sleep(20);
#pragma nounroll
    for (int spins = 0; spins < (1 << 18); spins++)
      if (__all(__hip_atomic_load(fp, __ATOMIC_RELAXED, __HIP_MEMORY_SCOPE_AGENT) == tag)) break;
  }
  SCHED;
  BAR;
  const unsigned hv = hl + (q * 2 + mb) * 2 * HP, hs = r2 * 14 * 16 * HP;
  u32x4 hf[14][2];
  auto load_h = [&](int st) {
    hf[st][0] = __builtin_amdgcn_raw_buffer_load_b128(hrs, hv, hs + st * 16 * HP, SC1);
    hf[st][1] = __builtin_amdgcn_raw_buffer_load_b128(hrs, hv + HP, hs + st * 16 * HP, SC1);
  };
#pragma unroll
  for (int st = 0; st < DEP; st++) load_h(st);
  f32x4 acc[4][2] = {};
  unsigned ro[2], rr[2][2], ov;
  auto put = [&](int c, int m) { *(LAS f32x4*)(sm + ro[c & 1] + m * 4096 + (c >> 1) * 128) = acc[c][m]; };
#pragma unroll
  for (int st = 0; st < 14; st++) {
    SCHED;
    if (st == 7) LG_BAR;
#pragma unroll
    for (int m = 0; m < 2; m++)
#pragma unroll
      for (int c = 0; c < 4; c++) {
        acc[c][m] = MFMA(af[st][c], __builtin_bit_cast(bf16x8, hf[st][m]), acc[c][m]);
        SCHED;
        if (st >= 3 && st < 13 && m == 0) { read_w(st + 1, c); SCHED; }
        if (st == 13 && m == 1) { put(c, 0); SCHED; }
      }
    if (st + DEP < 14) load_h(st + DEP);
    if (st == 10) {
      ro[0] = ((0x3052 >> (4 * q)) & 15) * UNIT + (32 * mb + (l & 15)) * 256 + ((g ^ (l & 7)) * 16);
      ro[1] = ro[0] ^ 64;
      rr[0][0] = (8 * w + (l >> 3)) * 256 + (((2 * (l & 7)) ^ (l >> 3)) * 16);
      rr[0][1] = rr[0][0] ^ 16; rr[1][0] = rr[0][0] + 5 * UNIT; rr[1][1] = rr[0][1] + 5 * UNIT;
      ov = (r2 * 64 + 8 * w + (l >> 3)) * 1024 + c2 * 128 + (l & 7) * 16;
      asm volatile("" : "+v"(ro[0]), "+v"(ro[1]), "+v"(rr[0][0]), "+v"(rr[0][1]), "+v"(rr[1][0]), "+v"(rr[1][1]), "+v"(ov));
    }
  }
  SCHED;
#pragma unroll
  for (int c = 0; c < 4; c++) put(c, 1);
  SCHED;
  LG_BAR;
  f32x4 s[4][2];
#pragma unroll
  for (int k = 0; k < 4; k++)
#pragma unroll
    for (int h = 0; h < 2; h++) s[k][h] = *(LAS f32x4*)(sm + rr[k == 3][h] + (k == 1 ? 2 : k == 2 ? 3 : 0) * UNIT);
  SCHED;
  const f32x4 s0 = (s[0][0] + s[1][0]) + (s[2][0] + s[3][0]), s1 = (s[0][1] + s[1][1]) + (s[2][1] + s[3][1]);
  const u32x4 o = {pk2((f32x2){s0[0], s0[1]}), pk2((f32x2){s0[2], s0[3]}), pk2((f32x2){s1[0], s1[1]}), pk2((f32x2){s1[2], s1[3]})};
  __builtin_amdgcn_raw_buffer_store_b128(o, __builtin_amdgcn_make_buffer_rsrc(out, 0, 2048 * 1024, 0x00020000), ov, 0, SC1);
}

extern "C" void kernel_launch(void* const* d_in, const int* in_sizes, int n_in, void* d_out, int out_size,
                              void* d_ws, size_t ws_size, hipStream_t stream) {
  mlp_fused<<<256, 768, 0, stream>>>((const char*)d_in[0], (const char*)d_in[1], (const char*)d_in[2],
                                     (const char*)d_in[3], (char*)d_out, (char*)d_ws);
}
```
